```python
import torch

def _softmax(x, axis=-1):
    return torch.softmax(x, dim=axis)
def _gelu(x):
    return torch.nn.functional.gelu(x, approximate="tanh")
def _silu(x):
    return torch.nn.functional.silu(x)
def _sigmoid(x):
    return torch.sigmoid(x)
def _layernorm(x, axis=-1, eps=1e-5):
    m = x.mean(dim=axis, keepdim=True)
    v = x.var(dim=axis, keepdim=True, unbiased=False)
    return (x - m) / torch.sqrt(v + eps)
def _scatter_add(tgt, idx, src, axis=0):
    return tgt.clone().index_add_(axis, idx, src)
def _segment_sum(src, seg, n):
    out = torch.zeros((n,) + src.shape[1:], dtype=src.dtype)
    return out.index_add_(0, seg, src)
def _pad(x, pw):
    flat = tuple(v for pair in reversed(pw) for v in pair)
    return torch.nn.functional.pad(x, flat)

kernel_name = 'interleave_layers__ssm_conv_s4d__grouped_kv_mqa__prompt'

def setup_inputs():

    _w = (lambda a: (lambda t: t.to(torch.bfloat16) if t.is_floating_point() else t)(torch.as_tensor(a)))
    def _A_setup():
        Bsz, L, D, N = 1, 2048, 640, 128
        return {
            "u": _w((lambda *s: torch.randn(*s, dtype=torch.bfloat16) * 0.05)(Bsz, L, D)),
            "log_A_real": _w((lambda *s: torch.randn(*s, dtype=torch.bfloat16) * 3.2)(D, N) * 0.5),
            "A_imag": _w((lambda *s: torch.randn(*s, dtype=torch.bfloat16) * 0.05)(D, N) * 3.14),
            "C_real": _w((lambda *s: torch.randn(*s, dtype=torch.bfloat16) * 0.05)(D, N) / N ** 0.5),
            "C_imag": _w((lambda *s: torch.randn(*s, dtype=torch.bfloat16) * 0.05)(D, N) / N ** 0.5),
            "log_dt": _w((lambda *s: torch.randn(*s, dtype=torch.bfloat16) * 0.05)(D) * 0.5 - 4.0),
            "D_skip": _w((lambda *s: torch.randn(*s, dtype=torch.bfloat16) * 0.05)(D)),
        }
    _A_in = _A_setup()

    def _B_setup():
        B, S, D, H = 1, 2048, 640, 32
        Dh = D // H
        return {
            "x": _w((lambda *s: torch.randn(*s, dtype=torch.bfloat16) * 0.05)(B, S, D)),
            "wq": _w((lambda *s: torch.randn(*s, dtype=torch.bfloat16) * 0.05)(D, H * Dh)),
            "wk": _w((lambda *s: torch.randn(*s, dtype=torch.bfloat16) * 0.05)(D, Dh)),
            "wv": _w((lambda *s: torch.randn(*s, dtype=torch.bfloat16) * 0.05)(D, Dh)),
            "wo": _w((lambda *s: torch.randn(*s, dtype=torch.bfloat16) * 0.05)(H * Dh, D)),
        }
    _B_in = _B_setup()

    return {
        "A_u": _A_in["u"],
        "A_log_A_real": _A_in["log_A_real"],
        "A_A_imag": _A_in["A_imag"],
        "A_C_real": _A_in["C_real"],
        "A_C_imag": _A_in["C_imag"],
        "A_log_dt": _A_in["log_dt"],
        "A_D_skip": _A_in["D_skip"],
        "B_x": _B_in["x"],
        "B_wq": _B_in["wq"],
        "B_wk": _B_in["wk"],
        "B_wv": _B_in["wv"],
        "B_wo": _B_in["wo"],
    }

def reference(A_u, A_log_A_real, A_A_imag, A_C_real, A_C_imag, A_log_dt, A_D_skip, B_x, B_wq, B_wk, B_wv, B_wo):
    def _A_ref(u, log_A_real, A_imag, C_real, C_imag, log_dt, D_skip):
        L = u.shape[1]
        dt = torch.exp(log_dt)
        A = -torch.exp(log_A_real) + 1j * A_imag
        C = (C_real + 1j * C_imag) * dt[:, None]
        pos = (lambda x, dt: x.to(dt))(torch.arange(L), log_dt.dtype)
        P = torch.exp((A * dt[:, None])[:, :, None] * pos[None, None, :])
        K = 2.0 * torch.einsum("dn,dnl->dl", C, P).real
        L2 = 2 * L
        u_t = (lambda x, *d: x.permute(*d))(u, 0, 2, 1)
        Kf = (lambda x, *a, **k: torch.fft.rfft(x.float(), *a, **k))(K, L2)
        Uf = (lambda x, *a, **k: torch.fft.rfft(x.float(), *a, **k))(u_t, L2)
        y = (lambda x, *a, **k: torch.fft.irfft(x, *a, **k).to(torch.bfloat16))(Uf * Kf[None, :, :], L2)[:, :, :L]
        y = (lambda x, *d: x.permute(*d))(y, 0, 2, 1)
        return y + u * D_skip[None, None, :]

    def _B_ref(x, wq, wk, wv, wo):
        B, S, D = x.shape
        H = 32
        Dh = D // H
        q = (lambda x, *d: x.permute(*d))((x @ wq).reshape(B, S, H, Dh), 0, 2, 1, 3)
        k = x @ wk
        v = x @ wv
        scores = torch.einsum("bhqd,bkd->bhqk", q, k) * (Dh ** -0.5)
        p = _softmax(scores, axis=-1)
        o = torch.einsum("bhqk,bkd->bhqd", p, v)
        return (lambda x, *d: x.permute(*d))(o, 0, 2, 1, 3).reshape(B, S, H * Dh) @ wo

    x = A_u
    x = x + _A_ref(_layernorm(x, axis=-1), A_log_A_real, A_A_imag, A_C_real, A_C_imag, A_log_dt, A_D_skip)
    x = x + _B_ref(_layernorm(x, axis=-1), B_wq, B_wk, B_wv, B_wo)
    x = x + _A_ref(_layernorm(x, axis=-1), A_log_A_real, A_A_imag, A_C_real, A_C_imag, A_log_dt, A_D_skip)
    x = x + _B_ref(_layernorm(x, axis=-1), B_wq, B_wk, B_wv, B_wo)
    return x
```

```cpp
#include <hip/hip_runtime.h>
#include <hip/hip_bf16.h>
#include <stdint.h>

typedef __attribute__((ext_vector_type(8))) __bf16 bf16x8;
typedef __attribute__((ext_vector_type(2))) __bf16 bf16x2;
typedef __attribute__((ext_vector_type(4))) short s16x4;
typedef __attribute__((ext_vector_type(16))) float f32x16;
typedef __attribute__((ext_vector_type(4))) float f32x4;
typedef __attribute__((ext_vector_type(2))) float f32x2;
typedef __attribute__((ext_vector_type(4))) unsigned u32x4;
typedef __attribute__((ext_vector_type(2))) unsigned u32x2;
typedef unsigned short u16;
#define DI __device__ __forceinline__

constexpr int L = 2048, D = 640, NS = 128, H = 32, DH = 20;
constexpr int RG_STRIDE = 2112;

constexpr size_t SZ_XT = (size_t)D * L * 2;
constexpr size_t WS_RG = 0;
constexpr size_t WS_XT0 = WS_RG + (size_t)D * RG_STRIDE * 2;
constexpr size_t WS_XT1 = WS_XT0 + SZ_XT;
constexpr size_t WS_XT2 = WS_XT1 + SZ_XT;
constexpr size_t WS_XT3 = WS_XT2 + SZ_XT;
constexpr size_t WS_ST0 = WS_XT3 + SZ_XT;
constexpr size_t WS_ST2 = WS_ST0 + (size_t)L * 8;
constexpr size_t WS_WQ = WS_ST2 + (size_t)L * 4 * 8;
constexpr size_t WS_WO = WS_WQ + (size_t)44 * 20 * 1024;
constexpr size_t WS_Q = WS_WO + (size_t)40 * 20 * 1024;
constexpr size_t WS_KV = WS_Q + SZ_XT;
constexpr size_t WS_O = WS_KV + (size_t)64 * 4096;
constexpr size_t WS_END = WS_O + SZ_XT;

DI float bf2f(u16 v) { return __uint_as_float(((unsigned)v) << 16); }
DI float bflo(unsigned w) { return __uint_as_float(w << 16); }
DI float bfhi(unsigned w) { return __uint_as_float(w & 0xffff0000u); }
DI unsigned pk2(float a, float b) {
  f32x2 f = {a, b};
  bf16x2 h = __builtin_convertvector(f, bf16x2);
  return __builtin_bit_cast(unsigned, h);
}
DI float rbf(float a) {
  return __uint_as_float(pk2(a, 0.f) << 16);
}
DI int crow(int reg, int h) { return (reg & 3) + 8 * (reg >> 2) + 4 * h; }
typedef __attribute__((address_space(3))) s16x4 lds_s16x4;
DI s16x4 tr_read(const char* p) { return __builtin_amdgcn_ds_read_tr16_b64_v4i16((lds_s16x4*)p); }

DI void cexpf_(float re, float im, float p, float& er, float& ei) {
  float ar = re * p, ai = im * p;
  float mag = __builtin_amdgcn_exp2f(ar * 1.4426950408889634f);
  float n = __builtin_rintf(ai * 0.15915494309189535f);
  float r = __builtin_fmaf(-n, 6.2831854820251465f, ai);
  r = __builtin_fmaf(-n, -1.7484555314695172e-07f, r);
  float rev = r * 0.15915494309189535f;
  er = mag * __builtin_amdgcn_cosf(rev);
  ei = mag * __builtin_amdgcn_sinf(rev);
}

DI void ln_params(float sum, float m2, float& mb, float& sb) {
  mb = rbf(sum * (1.0f / D));
  float vb = rbf(m2 * (1.0f / D));
  sb = rbf(__builtin_sqrtf(rbf(vb + 1e-5f)));
}

constexpr int PREP_KGEN = 640, PREP_LN = 64, PREP_WQ = 220, PREP_WO = 200, PREP_KV = 16;
constexpr int PREP_BLOCKS = PREP_KGEN + PREP_LN + PREP_WQ + PREP_WO + PREP_KV;

__global__ __launch_bounds__(256) void prep_kernel(const u16* __restrict__ xu, const u16* __restrict__ lar,
                                                   const u16* __restrict__ aim, const u16* __restrict__ cre,
                                                   const u16* __restrict__ cim, const u16* __restrict__ ldt,
                                                   const u16* __restrict__ wq, const u16* __restrict__ wk,
                                                   const u16* __restrict__ wv, const u16* __restrict__ wo,
                                                   char* __restrict__ ws) {
  __shared__ __attribute__((aligned(16))) char smem[32 * 1296 + 64];
  const int tid = threadIdx.x, lane = tid & 63, w = tid >> 6;
  int bid = blockIdx.x;
  if (bid < PREP_KGEN) {
    const int d = bid;
    f32x4* prm = (f32x4*)smem;
    float* part = (float*)(smem + 2048);
    float* last = (float*)(smem + 2048 + 16384);
    float* ktab = (float*)(smem + 2048 + 16384 + 64);
    const float dt = rbf(expf(bf2f(ldt[d])));
    if (tid < NS) {
      float are = -rbf(expf(bf2f(lar[d * NS + tid])));
      float ai = bf2f(aim[d * NS + tid]);
      f32x4 p = {are * dt, ai * dt, bf2f(cre[d * NS + tid]) * dt, bf2f(cim[d * NS + tid]) * dt};
      prm[tid] = p;
    }
    __syncthreads();
    const int i = lane & 15, kq = lane >> 4;
    f32x4 acc0 = {}, acc1 = {}, acc2 = {}, acc3 = {};
    float lastacc = 0.f;
#pragma unroll 2
    for (int t = 0; t < 8; ++t) {
      const int n = 32 * w + 4 * t + kq;
      const f32x4 p = prm[n];
      float gr, gi, br, bi;
      cexpf_(p[0], p[1], (float)(16 * i), gr, gi);
      cexpf_(p[0], p[1], (float)i, br, bi);
#define SEG(ACC)                                                          \
  {                                                                       \
    float ar = p[2] * gr - p[3] * gi, ai_ = p[2] * gi + p[3] * gr;        \
    ACC = __builtin_amdgcn_mfma_f32_16x16x4f32(ar, br, ACC, 0, 0, 0);     \
    ACC = __builtin_amdgcn_mfma_f32_16x16x4f32(-ai_, bi, ACC, 0, 0, 0);   \
  }
#define SQ()                                                              \
  {                                                                       \
    float t0 = gr * gr - gi * gi; gi = 2.f * gr * gi; gr = t0;            \
    float t1 = br * br - bi * bi; bi = 2.f * br * bi; br = t1;            \
  }
      SEG(acc0); SQ(); SEG(acc1); SQ(); SEG(acc2); SQ(); SEG(acc3);
      float t0 = gr * gr - gi * gi, t1 = 2.f * gr * gi;
      lastacc += p[2] * t0 - p[3] * t1;
#undef SEG
#undef SQ
    }
#pragma unroll
    for (int r = 0; r < 4; ++r) {
      const int idx = (4 * kq + r) * 16 + i;
      part[(w * 4 + 0) * 256 + idx] = acc0[r];
      part[(w * 4 + 1) * 256 + idx] = acc1[r];
      part[(w * 4 + 2) * 256 + idx] = acc2[r];
      part[(w * 4 + 3) * 256 + idx] = acc3[r];
    }
    {
      float v = (i == 8) ? lastacc : 0.f;
      v += __shfl_xor(v, 16);
      v += __shfl_xor(v, 32);
      if (lane == 8) last[w] = v;
    }
    __syncthreads();
#pragma unroll
    for (int s = 0; s < 4; ++s) {
      float v = part[(0 * 4 + s) * 256 + tid] + part[(1 * 4 + s) * 256 + tid] + part[(2 * 4 + s) * 256 + tid] +
                part[(3 * 4 + s) * 256 + tid];
      ktab[s * 256 + tid] = 2.f * v;
    }
    if (tid == 0) ktab[1024] = 2.f * (last[0] + last[1] + last[2] + last[3]);
    __syncthreads();
    u16* rg = (u16*)(ws + WS_RG) + (size_t)d * RG_STRIDE;
    for (int q = tid; q < RG_STRIDE / 8; q += 256) {
      float v[8];
#pragma unroll
      for (int e = 0; e < 8; ++e) {
        const int m = 8 * q + e;
        const int l = 2047 - m;
        float val = 0.f;
        if (l >= 0) {
          int p = (int)rbf((float)l);
          int idx;
          if (p < 256) idx = p;
          else if (p < 512) idx = 256 + (p >> 1);
          else if (p < 1024) idx = 512 + (p >> 2);
          else if (p < 2048) idx = 768 + (p >> 3);
          else idx = 1024;
          val = ktab[idx];
        }
        v[e] = val;
      }
      u32x4 o = {pk2(v[0], v[1]), pk2(v[2], v[3]), pk2(v[4], v[5]), pk2(v[6], v[7])};
      *(u32x4*)(rg + 8 * q) = o;
    }
    return;
  }
  bid -= PREP_KGEN;
  if (bid < PREP_LN) {
    const int l0 = bid * 32;
    const int row = tid >> 3, t8 = tid & 7;
    const u16* src = xu + (size_t)(l0 + row) * D;
    u32x4 v[10];
#pragma unroll
    for (int i = 0; i < 10; ++i) v[i] = *(const u32x4*)(src + (t8 + 8 * i) * 8);
    float s = 0.f;
#pragma unroll
    for (int i = 0; i < 10; ++i) {
#pragma unroll
      for (int j = 0; j < 4; ++j) s += bflo(v[i][j]) + bfhi(v[i][j]);
      *(u32x4*)(smem + row * 1296 + (t8 + 8 * i) * 16) = v[i];
    }
    s += __shfl_xor(s, 1); s += __shfl_xor(s, 2); s += __shfl_xor(s, 4);
    const float mean = s * (1.0f / D);
    float m2 = 0.f;
#pragma unroll
    for (int i = 0; i < 10; ++i)
#pragma unroll
      for (int j = 0; j < 4; ++j) {
        float a = bflo(v[i][j]) - mean, b = bfhi(v[i][j]) - mean;
        m2 += a * a + b * b;
      }
    m2 += __shfl_xor(m2, 1); m2 += __shfl_xor(m2, 2); m2 += __shfl_xor(m2, 4);
    if (t8 == 0) {
      f32x2 o = {s, m2};
      *(f32x2*)(ws + WS_ST0 + (size_t)(l0 + row) * 8) = o;
    }
    __syncthreads();
    u16* xt = (u16*)(ws + WS_XT0);
    const int g = lane >> 4, q = (lane & 15) >> 2, p = lane & 3, i16 = lane & 15;
    for (int cb = w; cb < 40; cb += 4) {
      const char* base = smem + (8 * g + q) * 1296 + cb * 32 + 8 * p;
      s16x4 lo = tr_read(base);
      s16x4 hi = tr_read(base + 4 * 1296);
      u32x2 a = __builtin_bit_cast(u32x2, lo), b = __builtin_bit_cast(u32x2, hi);
      u32x4 o = {a[0], a[1], b[0], b[1]};
      *(u32x4*)(xt + (size_t)(cb * 16 + i16) * L + l0 + 8 * g) = o;
    }
    return;
  }
  bid -= PREP_LN;
  if (bid < PREP_WQ + PREP_WO) {
    const bool isq = bid < PREP_WQ;
    const int f = (isq ? bid : bid - PREP_WQ) * 4 + w;
    const int nt = f / 20, ks = f % 20;
    const int n = (lane & 15), k0 = 32 * ks + 8 * (lane >> 4);
    const u16* src;
    int ld, col;
    bool valid = true;
    if (!isq) { src = wo; ld = D; col = 16 * nt + n; }
    else if (nt < 40) { src = wq; ld = D; col = 16 * nt + n; }
    else if (nt < 42) { src = wk; ld = DH; col = 16 * (nt - 40) + n; valid = col < DH; }
    else { src = wv; ld = DH; col = 16 * (nt - 42) + n; valid = col < DH; }
    unsigned e[8];
#pragma unroll
    for (int j = 0; j < 8; ++j) e[j] = valid ? (unsigned)src[(size_t)(k0 + j) * ld + col] : 0u;
    u32x4 o = {e[0] | (e[1] << 16), e[2] | (e[3] << 16), e[4] | (e[5] << 16), e[6] | (e[7] << 16)};
    *(u32x4*)(ws + (isq ? WS_WQ : WS_WO) + (size_t)f * 1024 + lane * 16) = o;
    return;
  }
  bid -= PREP_WQ + PREP_WO;
  {
    for (int c = bid * 256 + tid; c < 64 * 256; c += PREP_KV * 256) {
      const int fr = (c >> 6) & 3, ln = c & 63;
      unsigned v = (fr >= 2 && (ln & 31) == DH) ? 0x3f803f80u : 0u;
      u32x4 o = {v, v, v, v};
      *(u32x4*)(ws + WS_KV + (size_t)c * 16) = o;
    }
  }
}

constexpr int CS = 4160;
template <int NP>
__global__ __launch_bounds__(256) void conv_kernel(const u16* __restrict__ xt, const f32x2* __restrict__ stats,
                                                   const u16* __restrict__ rg, const u16* __restrict__ dskip,
                                                   u16* __restrict__ xout) {
  __shared__ __attribute__((aligned(16))) char smem[8 * CS + 96 * 80];
  char* KC = smem;
  char* U = smem + 8 * CS;
  const int tid = threadIdx.x, lane = tid & 63, w = tid >> 6;
  const int d = blockIdx.x;
  const u32x4 xv = *(const u32x4*)(xt + (size_t)d * L + 8 * tid);
  const u16* rgd = rg + (size_t)d * RG_STRIDE;
  const u32x4 r0 = *(const u32x4*)(rgd + 8 * tid);
  const u32x4 r1 = *(const u32x4*)(rgd + 8 * tid + 8);
  const float dsk = bf2f(dskip[d]);
  float mb[8], sb[8];
#pragma unroll
  for (int e = 0; e < 8; ++e) {
    const int l = 8 * tid + e;
    float sum = 0.f, m2 = 0.f;
    if (NP == 1) {
      f32x2 s = stats[l];
      sum = s[0]; m2 = s[1];
    } else {
      f32x2 s[NP];
#pragma unroll
      for (int p = 0; p < NP; ++p) { s[p] = stats[l * NP + p]; sum += s[p][0]; }
      const float mean = sum * (1.0f / D);
#pragma unroll
      for (int p = 0; p < NP; ++p) {
        float dm = s[p][0] * ((float)NP / D) - mean;
        m2 += s[p][1] + dm * dm * ((float)D / NP);
      }
    }
    ln_params(sum, m2, mb[e], sb[e]);
  }
  {
    unsigned wv[8] = {r0[0], r0[1], r0[2], r0[3], r1[0], r1[1], r1[2], r1[3]};
#pragma unroll
    for (int s = 0; s < 8; ++s) {
      u32x4 o;
#pragma unroll
      for (int k = 0; k < 4; ++k) {
        if (s & 1) o[k] = __builtin_amdgcn_alignbit(wv[(s >> 1) + k + 1], wv[(s >> 1) + k], 16);
        else o[k] = wv[(s >> 1) + k];
      }
      *(u32x4*)(KC + s * CS + tid * 16) = o;
    }
    if (tid < 32) {
      u32x4 z = {0u, 0u, 0u, 0u};
      *(u32x4*)(KC + (tid >> 2) * CS + (256 + (tid & 3)) * 16) = z;
    }
  }
  float xf[8], uf[8];
#pragma unroll
  for (int j = 0; j < 4; ++j) { xf[2 * j] = bflo(xv[j]); xf[2 * j + 1] = bfhi(xv[j]); }
#pragma unroll
  for (int e = 0; e < 8; ++e) uf[e] = rbf(rbf(xf[e] - mb[e]) / sb[e]);
  {
    u32x4 o = {pk2(uf[0], uf[1]), pk2(uf[2], uf[3]), pk2(uf[4], uf[5]), pk2(uf[6], uf[7])};
    *(u32x4*)(U + ((tid >> 2) + 32) * 80 + (tid & 3) * 16) = o;
    if (tid < 128) {
      u32x4 z = {0u, 0u, 0u, 0u};
      *(u32x4*)(U + (tid >> 2) * 80 + (tid & 3) * 16) = z;
    }
  }
  __syncthreads();
  const int b = lane & 31, h = lane >> 5;
  const char* kbase = KC + (7 - (b & 7)) * CS + 16 * (255 - (b >> 3) + h);
  const char* ubase = U + (b + 32) * 80 + 16 * h;
  f32x16 acc0 = {}, acc1 = {};
  int c0, c1, c2;
  if (w == 0) { c0 = 0; c1 = 12; c2 = 12; }
  else if (w == 1) { c0 = 12; c1 = 24; c2 = 24; }
  else if (w == 2) { c0 = 24; c1 = 32; c2 = 40; }
  else { c0 = 40; c1 = 40; c2 = 64; }
  for (int c = c0; c < c1; ++c) {
#pragma unroll
    for (int ks = 0; ks < 2; ++ks) {
      const bf16x8 kf = *(const bf16x8*)(kbase - 64 * c + 32 * ks);
      const bf16x8 u0 = *(const bf16x8*)(ubase - 80 * c + 32 * ks);
      const bf16x8 u1 = *(const bf16x8*)(ubase + 80 * (32 - c) + 32 * ks);
      acc0 = __builtin_amdgcn_mfma_f32_32x32x16_bf16(u0, kf, acc0, 0, 0, 0);
      acc1 = __builtin_amdgcn_mfma_f32_32x32x16_bf16(u1, kf, acc1, 0, 0, 0);
    }
  }
  for (int c = c1; c < c2; ++c) {
#pragma unroll
    for (int ks = 0; ks < 2; ++ks) {
      const bf16x8 kf = *(const bf16x8*)(kbase - 64 * c + 32 * ks);
      const bf16x8 u1 = *(const bf16x8*)(ubase + 80 * (32 - c) + 32 * ks);
      acc1 = __builtin_amdgcn_mfma_f32_32x32x16_bf16(u1, kf, acc1, 0, 0, 0);
    }
  }
  __syncthreads();
  float* part = (float*)smem;
#pragma unroll
  for (int r = 0; r < 16; ++r) {
    const int a = crow(r, h);
    part[w * 2048 + 32 * a + b] = acc0[r];
    part[w * 2048 + 1024 + 32 * a + b] = acc1[r];
  }
  __syncthreads();
  float y[8];
#pragma unroll
  for (int e = 0; e < 8; ++e) y[e] = 0.f;
#pragma unroll
  for (int ww = 0; ww < 4; ++ww) {
    const f32x4 p0 = *(const f32x4*)(part + ww * 2048 + 8 * tid);
    const f32x4 p1 = *(const f32x4*)(part + ww * 2048 + 8 * tid + 4);
#pragma unroll
    for (int e = 0; e < 4; ++e) { y[e] += p0[e]; y[4 + e] += p1[e]; }
  }
  float o[8];
#pragma unroll
  for (int e = 0; e < 8; ++e) o[e] = xf[e] + rbf(rbf(y[e]) + rbf(uf[e] * dsk));
  u32x4 ov = {pk2(o[0], o[1]), pk2(o[2], o[3]), pk2(o[4], o[5]), pk2(o[6], o[7])};
  *(u32x4*)(xout + (size_t)d * L + 8 * tid) = ov;
}

__global__ __launch_bounds__(256) void qkv_kernel(const u16* __restrict__ xt, const char* __restrict__ wf,
                                                  u16* __restrict__ Q, char* __restrict__ kvf) {
  __shared__ __attribute__((aligned(16))) char smem[640 * 64 + 1024];
  float* red = (float*)(smem + 640 * 64);
  const int tid = threadIdx.x, lane = tid & 63, w = tid >> 6;
  const int rt = blockIdx.x >> 2, ns = blockIdx.x & 3;
  const int l0 = 32 * rt;
  const int dsub = lane >> 2, c = lane & 3;
  u32x4 v[10];
#pragma unroll
  for (int i = 0; i < 10; ++i) v[i] = *(const u32x4*)(xt + (size_t)(64 * i + 16 * w + dsub) * L + l0 + 8 * c);
  float s[8];
#pragma unroll
  for (int e = 0; e < 8; ++e) s[e] = 0.f;
#pragma unroll
  for (int i = 0; i < 10; ++i)
#pragma unroll
    for (int j = 0; j < 4; ++j) { s[2 * j] += bflo(v[i][j]); s[2 * j + 1] += bfhi(v[i][j]); }
#pragma unroll
  for (int e = 0; e < 8; ++e) {
    s[e] += __shfl_xor(s[e], 4); s[e] += __shfl_xor(s[e], 8);
    s[e] += __shfl_xor(s[e], 16); s[e] += __shfl_xor(s[e], 32);
  }
  if (lane < 4) {
#pragma unroll
    for (int e = 0; e < 8; ++e) red[w * 32 + 8 * c + e] = s[e];
  }
  __syncthreads();
  float mean[8], sum[8];
#pragma unroll
  for (int e = 0; e < 8; ++e) {
    sum[e] = red[8 * c + e] + red[32 + 8 * c + e] + red[64 + 8 * c + e] + red[96 + 8 * c + e];
    mean[e] = sum[e] * (1.0f / D);
    s[e] = 0.f;
  }
#pragma unroll
  for (int i = 0; i < 10; ++i)
#pragma unroll
    for (int j = 0; j < 4; ++j) {
      float a = bflo(v[i][j]) - mean[2 * j], b = bfhi(v[i][j]) - mean[2 * j + 1];
      s[2 * j] += a * a; s[2 * j + 1] += b * b;
    }
#pragma unroll
  for (int e = 0; e < 8; ++e) {
    s[e] += __shfl_xor(s[e], 4); s[e] += __shfl_xor(s[e], 8);
    s[e] += __shfl_xor(s[e], 16); s[e] += __shfl_xor(s[e], 32);
  }
  if (lane < 4) {
#pragma unroll
    for (int e = 0; e < 8; ++e) red[128 + w * 32 + 8 * c + e] = s[e];
  }
  __syncthreads();
  float mb[8], sb[8];
#pragma unroll
  for (int e = 0; e < 8; ++e) {
    float m2 = red[128 + 8 * c + e] + red[160 + 8 * c + e] + red[192 + 8 * c + e] + red[224 + 8 * c + e];
    ln_params(sum[e], m2, mb[e], sb[e]);
  }
#pragma unroll
  for (int i = 0; i < 10; ++i) {
    const int d = 64 * i + 16 * w + dsub;
    u32x4 o;
#pragma unroll
    for (int j = 0; j < 4; ++j) {
      float a = rbf(bflo(v[i][j]) - mb[2 * j]) / sb[2 * j];
      float b = rbf(bfhi(v[i][j]) - mb[2 * j + 1]) / sb[2 * j + 1];
      o[j] = pk2(a, b);
    }
    *(u32x4*)(smem + d * 64 + ((c ^ (((d >> 3) & 1) << 1)) * 16)) = o;
  }
  __syncthreads();
  const int g = lane >> 4, q = (lane & 15) >> 2, p = lane & 3;
  const char* ub0 = smem + (8 * g + q) * 64 + 32 * (0 ^ (g & 1)) + 8 * p;
  const char* ub1 = smem + (8 * g + q) * 64 + 32 * (1 ^ (g & 1)) + 8 * p;
  const int nt0 = ns * 11 + w;
  const int ncnt = (w < 3) ? 3 : 2;
  f32x4 acc[3][2];
#pragma unroll
  for (int a = 0; a < 3; ++a) { acc[a][0] = f32x4{}; acc[a][1] = f32x4{}; }
  const char* wbase = wf + (size_t)nt0 * 20 * 1024 + lane * 16;
  const int wst2 = (ncnt == 3) ? 8 * 20 * 1024 : 4 * 20 * 1024;
#pragma unroll 4
  for (int ks = 0; ks < 20; ++ks) {
    const bf16x8 w0 = *(const bf16x8*)(wbase + ks * 1024);
    const bf16x8 w1 = *(const bf16x8*)(wbase + 4 * 20 * 1024 + ks * 1024);
    const bf16x8 w2 = *(const bf16x8*)(wbase + wst2 + ks * 1024);
    s16x4 a0 = tr_read(ub0 + ks * 2048), a1 = tr_read(ub0 + ks * 2048 + 256);
    s16x4 b0 = tr_read(ub1 + ks * 2048), b1 = tr_read(ub1 + ks * 2048 + 256);
    bf16x8 u0 = __builtin_bit_cast(bf16x8, __builtin_shufflevector(a0, a1, 0, 1, 2, 3, 4, 5, 6, 7));
    bf16x8 u1 = __builtin_bit_cast(bf16x8, __builtin_shufflevector(b0, b1, 0, 1, 2, 3, 4, 5, 6, 7));
    acc[0][0] = __builtin_amdgcn_mfma_f32_16x16x32_bf16(w0, u0, acc[0][0], 0, 0, 0);
    acc[0][1] = __builtin_amdgcn_mfma_f32_16x16x32_bf16(w0, u1, acc[0][1], 0, 0, 0);
    acc[1][0] = __builtin_amdgcn_mfma_f32_16x16x32_bf16(w1, u0, acc[1][0], 0, 0, 0);
    acc[1][1] = __builtin_amdgcn_mfma_f32_16x16x32_bf16(w1, u1, acc[1][1], 0, 0, 0);
    acc[2][0] = __builtin_amdgcn_mfma_f32_16x16x32_bf16(w2, u0, acc[2][0], 0, 0, 0);
    acc[2][1] = __builtin_amdgcn_mfma_f32_16x16x32_bf16(w2, u1, acc[2][1], 0, 0, 0);
  }
  const float qscale = 0.22360679774997896f * 1.4426950408889634f;
#pragma unroll
  for (int a = 0; a < 3; ++a) {
    if (a >= ncnt) break;
    const int nt = nt0 + 4 * a;
#pragma unroll
    for (int lt = 0; lt < 2; ++lt) {
      const int l = l0 + 16 * lt + (lane & 15);
      const f32x4 r = acc[a][lt];
      if (nt < 40) {
        u32x2 o = {pk2(r[0] * qscale, r[1] * qscale), pk2(r[2] * qscale, r[3] * qscale)};
        *(u32x2*)(Q + (size_t)l * D + 16 * nt + 4 * g) = o;
      } else if (nt < 42) {
        const int r32 = l & 31;
        u32x2 o = {pk2(r[0], r[1]), pk2(r[2], r[3])};
        *(u32x2*)(kvf + (size_t)rt * 4096 + (nt - 40) * 1024 + (r32 + 32 * (g >> 1)) * 16 + 8 * (g & 1)) = o;
      } else {
        const int r32 = l & 31;
        const int s_ = r32 >> 4, jh = (r32 >> 3) & 1, hh = (r32 >> 2) & 1, jl = r32 & 3;
        const int j = 4 * jh + jl;
#pragma unroll
        for (int e = 0; e < 4; ++e) {
          const int dv = 16 * (nt - 42) + 4 * g + e;
          if (dv < DH) {
            u16 bv = (u16)(pk2(r[e], 0.f) & 0xffffu);
            *(u16*)(kvf + (size_t)rt * 4096 + (2 + s_) * 1024 + (dv + 32 * hh) * 16 + 2 * j) = bv;
          }
        }
      }
    }
  }
}

constexpr int ACH = 8;
__global__ __launch_bounds__(512) void attn_kernel(const u16* __restrict__ Q, const char* __restrict__ kvf,
                                                   u16* __restrict__ O) {
  __shared__ __attribute__((aligned(16))) char smem[2 * ACH * 4096];
  const int tid = threadIdx.x, lane = tid & 63, w = tid >> 6;
  const int hq = lane & 31, h = lane >> 5;
  const int row = blockIdx.x * 8 + w;
  auto stage = [&](int chunk, int buf) {
#pragma unroll
    for (int i = 0; i < 4; ++i) {
      const char* src = kvf + (size_t)(chunk * ACH + w) * 4096 + i * 1024 + lane * 16;
      __builtin_amdgcn_global_load_lds((const __attribute__((address_space(1))) void*)src,
                                       (__attribute__((address_space(3))) void*)(smem + buf * ACH * 4096 + w * 4096 + i * 1024),
                                       16, 0, 0);
    }
  };
  stage(0, 0);
  bf16x8 qf0, qf1;
  {
    const u16* qp = Q + (size_t)row * D + hq * DH;
    u32x2 a = *(const u32x2*)(qp + 8 * h), b = *(const u32x2*)(qp + 8 * h + 4);
    u32x4 t = {a[0], a[1], b[0], b[1]};
    qf0 = __builtin_bit_cast(bf16x8, t);
    u32x2 c = *(const u32x2*)(qp + 16);
    u32x4 t1 = {h ? 0u : c[0], h ? 0u : c[1], 0u, 0u};
    qf1 = __builtin_bit_cast(bf16x8, t1);
  }
  constexpr float OFF = 7.f;
  f32x16 o = {};
  f32x16 negm = {};
  __syncthreads();
  for (int ch = 0; ch < 64 / ACH; ++ch) {
    if (ch + 1 < 64 / ACH) stage(ch + 1, (ch + 1) & 1);
    const char* base = smem + (ch & 1) * ACH * 4096 + lane * 16;
#pragma unroll 2
    for (int t = 0; t < ACH; ++t) {
      const bf16x8 k0 = *(const bf16x8*)(base + t * 4096);
      const bf16x8 k1 = *(const bf16x8*)(base + t * 4096 + 1024);
      const bf16x8 v0 = *(const bf16x8*)(base + t * 4096 + 2048);
      const bf16x8 v1 = *(const bf16x8*)(base + t * 4096 + 3072);
      f32x16 s = __builtin_amdgcn_mfma_f32_32x32x16_bf16(k0, qf0, negm, 0, 0, 0);
      s = __builtin_amdgcn_mfma_f32_32x32x16_bf16(k1, qf1, s, 0, 0, 0);
      unsigned pk[8];
#pragma unroll
      for (int r = 0; r < 8; ++r) pk[r] = pk2(__builtin_amdgcn_exp2f(s[2 * r]), __builtin_amdgcn_exp2f(s[2 * r + 1]));
      const unsigned orv = (pk[0] | pk[1] | pk[2]) | (pk[3] | pk[4] | pk[5]) | (pk[6] | pk[7]);
      const bool first = (ch == 0 && t == 0);
      if (__builtin_expect(__any((orv & 0x40004000u) != 0u) || first, 0)) {
        float tm = s[0];
#pragma unroll
        for (int r = 1; r < 16; ++r) tm = fmaxf(tm, s[r]);
        tm = fmaxf(tm, __shfl_xor(tm, 32));
        float delta = tm + OFF;
        if (!first) delta = fmaxf(delta, 0.f);
        const float sc = first ? 0.f : __builtin_amdgcn_exp2f(-delta);
#pragma unroll
        for (int r = 0; r < 16; ++r) { o[r] *= sc; negm[r] -= delta; }
#pragma unroll
        for (int r = 0; r < 8; ++r)
          pk[r] = pk2(__builtin_amdgcn_exp2f(s[2 * r] - delta), __builtin_amdgcn_exp2f(s[2 * r + 1] - delta));
      }
      u32x4 pa = {pk[0], pk[1], pk[2], pk[3]}, pb = {pk[4], pk[5], pk[6], pk[7]};
      o = __builtin_amdgcn_mfma_f32_32x32x16_bf16(v0, __builtin_bit_cast(bf16x8, pa), o, 0, 0, 0);
      o = __builtin_amdgcn_mfma_f32_32x32x16_bf16(v1, __builtin_bit_cast(bf16x8, pb), o, 0, 0, 0);
    }
    __syncthreads();
  }
  float lsum = o[8];
  lsum = __shfl(lsum, hq + 32);
  const float inv = 1.0f / lsum;
  u16* op = O + (size_t)row * D + hq * DH;
#pragma unroll
  for (int g = 0; g < 3; ++g) {
    const int dv = 8 * g + 4 * h;
    if (dv < DH) {
      u32x2 ov = {pk2(o[4 * g] * inv, o[4 * g + 1] * inv), pk2(o[4 * g + 2] * inv, o[4 * g + 3] * inv)};
      *(u32x2*)(op + dv) = ov;
    }
  }
}

template <bool FINAL>
__global__ __launch_bounds__(320) void oproj_kernel(const u16* __restrict__ O, const char* __restrict__ wf,
                                                    const u16* __restrict__ xres, u16* __restrict__ xout,
                                                    f32x2* __restrict__ stats) {
  __shared__ __attribute__((aligned(16))) char smem[32 * 1296];
  const int tid = threadIdx.x, lane = tid & 63, w = tid >> 6;
  const int rt = blockIdx.x >> 2, ns = blockIdx.x & 3;
  const int l0 = 32 * rt;
  for (int c = tid; c < 32 * 80; c += 320) {
    const int r = c / 80, cc = c % 80;
    *(u32x4*)(smem + r * 1296 + cc * 16) = *(const u32x4*)(O + (size_t)(l0 + r) * D + cc * 8);
  }
  __syncthreads();
  const int g = lane >> 4, i16 = lane & 15;
  const int nt0 = ns * 10 + 2 * w;
  const char* wbase = wf + (size_t)nt0 * 20 * 1024 + lane * 16;
  const char* obase = smem + i16 * 1296 + 16 * g;
  f32x4 acc[2][2];
  acc[0][0] = f32x4{}; acc[0][1] = f32x4{}; acc[1][0] = f32x4{}; acc[1][1] = f32x4{};
#pragma unroll 4
  for (int ks = 0; ks < 20; ++ks) {
    const bf16x8 w0 = *(const bf16x8*)(wbase + ks * 1024);
    const bf16x8 w1 = *(const bf16x8*)(wbase + 20 * 1024 + ks * 1024);
    const bf16x8 o0 = *(const bf16x8*)(obase + ks * 64);
    const bf16x8 o1 = *(const bf16x8*)(obase + 16 * 1296 + ks * 64);
    if (FINAL) {
      acc[0][0] = __builtin_amdgcn_mfma_f32_16x16x32_bf16(w0, o0, acc[0][0], 0, 0, 0);
      acc[0][1] = __builtin_amdgcn_mfma_f32_16x16x32_bf16(w0, o1, acc[0][1], 0, 0, 0);
      acc[1][0] = __builtin_amdgcn_mfma_f32_16x16x32_bf16(w1, o0, acc[1][0], 0, 0, 0);
      acc[1][1] = __builtin_amdgcn_mfma_f32_16x16x32_bf16(w1, o1, acc[1][1], 0, 0, 0);
    } else {
      acc[0][0] = __builtin_amdgcn_mfma_f32_16x16x32_bf16(o0, w0, acc[0][0], 0, 0, 0);
      acc[0][1] = __builtin_amdgcn_mfma_f32_16x16x32_bf16(o1, w0, acc[0][1], 0, 0, 0);
      acc[1][0] = __builtin_amdgcn_mfma_f32_16x16x32_bf16(o0, w1, acc[1][0], 0, 0, 0);
      acc[1][1] = __builtin_amdgcn_mfma_f32_16x16x32_bf16(o1, w1, acc[1][1], 0, 0, 0);
    }
  }
  if (FINAL) {
#pragma unroll
    for (int a = 0; a < 2; ++a)
#pragma unroll
      for (int lt = 0; lt < 2; ++lt) {
        const int l = l0 + 16 * lt + i16;
        const int n = 16 * (nt0 + a) + 4 * g;
        float r[4];
#pragma unroll
        for (int e = 0; e < 4; ++e) r[e] = bf2f(xres[(size_t)(n + e) * L + l]) + rbf(acc[a][lt][e]);
        u32x2 ov = {pk2(r[0], r[1]), pk2(r[2], r[3])};
        *(u32x2*)(xout + (size_t)l * D + n) = ov;
      }
  } else {
    __syncthreads();
    float* st = (float*)smem;
#pragma unroll
    for (int a = 0; a < 2; ++a)
#pragma unroll
      for (int lt = 0; lt < 2; ++lt) {
        const int n = 16 * (nt0 + a) + i16;
        const int l = l0 + 16 * lt + 4 * g;
        const u32x2 xr = *(const u32x2*)(xres + (size_t)n * L + l);
        float r[4];
        r[0] = rbf(bflo(xr[0]) + rbf(acc[a][lt][0]));
        r[1] = rbf(bfhi(xr[0]) + rbf(acc[a][lt][1]));
        r[2] = rbf(bflo(xr[1]) + rbf(acc[a][lt][2]));
        r[3] = rbf(bfhi(xr[1]) + rbf(acc[a][lt][3]));
        u32x2 ov = {pk2(r[0], r[1]), pk2(r[2], r[3])};
        *(u32x2*)(xout + (size_t)n * L + l) = ov;
        const int nl = 16 * (2 * w + a) + i16;
#pragma unroll
        for (int e = 0; e < 4; ++e) st[(16 * lt + 4 * g + e) * 164 + nl] = r[e];
      }
    __syncthreads();
    if (tid < 256) {
      const int l = tid >> 3, p8 = tid & 7;
      float v[20];
      float s = 0.f;
#pragma unroll
      for (int e = 0; e < 20; ++e) { v[e] = st[l * 164 + p8 + 8 * e]; s += v[e]; }
      s += __shfl_xor(s, 1); s += __shfl_xor(s, 2); s += __shfl_xor(s, 4);
      const float mean = s * (1.0f / 160.f);
      float m2 = 0.f;
#pragma unroll
      for (int e = 0; e < 20; ++e) { float a = v[e] - mean; m2 += a * a; }
      m2 += __shfl_xor(m2, 1); m2 += __shfl_xor(m2, 2); m2 += __shfl_xor(m2, 4);
      if (p8 == 0) {
        f32x2 o = {s, m2};
        stats[(size_t)(l0 + l) * 4 + ns] = o;
      }
    }
  }
}

extern "C" void kernel_launch(void* const* d_in, const int* in_sizes, int n_in, void* d_out, int out_size,
                              void* d_ws, size_t ws_size, hipStream_t stream) {
  const u16* xu = (const u16*)d_in[0];
  const u16* lar = (const u16*)d_in[1];
  const u16* aim = (const u16*)d_in[2];
  const u16* cre = (const u16*)d_in[3];
  const u16* cim = (const u16*)d_in[4];
  const u16* ldt = (const u16*)d_in[5];
  const u16* dsk = (const u16*)d_in[6];
  const u16* wq = (const u16*)d_in[8];
  const u16* wk = (const u16*)d_in[9];
  const u16* wv = (const u16*)d_in[10];
  const u16* wo = (const u16*)d_in[11];
  char* ws = (char*)d_ws;
  u16* out = (u16*)d_out;
  const u16* rg = (const u16*)(ws + WS_RG);
  u16* xt0 = (u16*)(ws + WS_XT0);
  u16* xt1 = (u16*)(ws + WS_XT1);
  u16* xt2 = (u16*)(ws + WS_XT2);
  u16* xt3 = (u16*)(ws + WS_XT3);
  f32x2* st0 = (f32x2*)(ws + WS_ST0);
  f32x2* st2 = (f32x2*)(ws + WS_ST2);
  u16* Q = (u16*)(ws + WS_Q);
  u16* O = (u16*)(ws + WS_O);
  char* kvf = ws + WS_KV;

  prep_kernel<<<PREP_BLOCKS, 256, 0, stream>>>(xu, lar, aim, cre, cim, ldt, wq, wk, wv, wo, ws);
  conv_kernel<1><<<D, 256, 0, stream>>>(xt0, st0, rg, dsk, xt1);
  qkv_kernel<<<256, 256, 0, stream>>>(xt1, ws + WS_WQ, Q, kvf);
  attn_kernel<<<256, 512, 0, stream>>>(Q, kvf, O);
  oproj_kernel<false><<<256, 320, 0, stream>>>(O, ws + WS_WO, xt1, xt2, st2);
  conv_kernel<4><<<D, 256, 0, stream>>>(xt2, st2, rg, dsk, xt3);
  qkv_kernel<<<256, 256, 0, stream>>>(xt3, ws + WS_WQ, Q, kvf);
  attn_kernel<<<256, 512, 0, stream>>>(Q, kvf, O);
  oproj_kernel<true><<<256, 320, 0, stream>>>(O, ws + WS_WO, xt3, out, nullptr);
}
```

```cpp
#include <hip/hip_runtime.h>
#include <hip/hip_bf16.h>
#include <stdint.h>

typedef __attribute__((ext_vector_type(8))) __bf16 bf16x8;
typedef __attribute__((ext_vector_type(2))) __bf16 bf16x2;
typedef __attribute__((ext_vector_type(4))) short s16x4;
typedef __attribute__((ext_vector_type(16))) float f32x16;
typedef __attribute__((ext_vector_type(4))) float f32x4;
typedef __attribute__((ext_vector_type(2))) float f32x2;
typedef __attribute__((ext_vector_type(4))) unsigned u32x4;
typedef __attribute__((ext_vector_type(2))) unsigned u32x2;
typedef unsigned short u16;
#define DI __device__ __forceinline__

constexpr int L = 2048, D = 640, NS = 128, H = 32, DH = 20;
constexpr int RG_STRIDE = 2112;

constexpr size_t SZ_XT = (size_t)D * L * 2;
constexpr size_t WS_RG = 0;
constexpr size_t WS_XT0 = WS_RG + (size_t)D * RG_STRIDE * 2;
constexpr size_t WS_XT1 = WS_XT0 + SZ_XT;
constexpr size_t WS_XT2 = WS_XT1 + SZ_XT;
constexpr size_t WS_XT3 = WS_XT2 + SZ_XT;
constexpr size_t WS_ST0 = WS_XT3 + SZ_XT;
constexpr size_t WS_ST2 = WS_ST0 + (size_t)L * 8;
constexpr size_t WS_WQ = WS_ST2 + (size_t)L * 4 * 8;
constexpr size_t WS_WO = WS_WQ + (size_t)44 * 20 * 1024;
constexpr size_t WS_Q = WS_WO + (size_t)40 * 20 * 1024;
constexpr size_t WS_KV = WS_Q + SZ_XT;
constexpr size_t WS_O = WS_KV + (size_t)64 * 4096;
constexpr size_t WS_END = WS_O + SZ_XT;

#define STAMP(kid, i) do {} while (0)
DI float bf2f(u16 v) { return __uint_as_float(((unsigned)v) << 16); }
DI float bflo(unsigned w) { return __uint_as_float(w << 16); }
DI float bfhi(unsigned w) { return __uint_as_float(w & 0xffff0000u); }
DI unsigned pk2(float a, float b) {
  f32x2 f = {a, b};
  bf16x2 h = __builtin_convertvector(f, bf16x2);
  return __builtin_bit_cast(unsigned, h);
}
DI float rbf(float a) {
  return __uint_as_float(pk2(a, 0.f) << 16);
}
DI int crow(int reg, int h) { return (reg & 3) + 8 * (reg >> 2) + 4 * h; }
typedef __attribute__((address_space(3))) s16x4 lds_s16x4;
DI s16x4 tr_read(const char* p) { return __builtin_amdgcn_ds_read_tr16_b64_v4i16((lds_s16x4*)p); }

DI void cexpf_(float re, float im, float p, float& er, float& ei) {
  float ar = re * p, ai = im * p;
  float mag = __builtin_amdgcn_exp2f(ar * 1.4426950408889634f);
  float n = __builtin_rintf(ai * 0.15915494309189535f);
  float r = __builtin_fmaf(-n, 6.2831854820251465f, ai);
  r = __builtin_fmaf(-n, -1.7484555314695172e-07f, r);
  float rev = r * 0.15915494309189535f;
  er = mag * __builtin_amdgcn_cosf(rev);
  ei = mag * __builtin_amdgcn_sinf(rev);
}

DI void ln_params(float sum, float m2, float& mb, float& sb) {
  mb = rbf(sum * (1.0f / D));
  float vb = rbf(m2 * (1.0f / D));
  sb = __builtin_amdgcn_rcpf(rbf(__builtin_amdgcn_sqrtf(rbf(vb + 1e-5f))));
}

constexpr int PREP_KGEN = 640, PREP_LN = 128, PREP_WQ = 220, PREP_WO = 200, PREP_KV = 16;
constexpr int PREP_BLOCKS = PREP_KGEN + PREP_LN + PREP_WQ + PREP_WO + PREP_KV;

DI void cexp_rev(float e0, float e1, float f0, float f1, float p, float& er, float& ei) {
  const float mag = __builtin_amdgcn_exp2f(__builtin_fmaf(e1, p, e0));
  const float rev = __builtin_amdgcn_fractf(__builtin_fmaf(f1, p, f0));
  er = mag * __builtin_amdgcn_cosf(rev);
  ei = mag * __builtin_amdgcn_sinf(rev);
}

__global__ __launch_bounds__(256) void prep_kernel(const u16* __restrict__ xu, const u16* __restrict__ lar,
                                                   const u16* __restrict__ aim, const u16* __restrict__ cre,
                                                   const u16* __restrict__ cim, const u16* __restrict__ ldt,
                                                   const u16* __restrict__ wq, const u16* __restrict__ wk,
                                                   const u16* __restrict__ wv, const u16* __restrict__ wo,
                                                   char* __restrict__ ws) {
  __shared__ __attribute__((aligned(16))) char smem[16 * 1296 + 64];
  const int tid = threadIdx.x, lane = tid & 63, w = tid >> 6;
  int bid = blockIdx.x;
  if (bid < PREP_KGEN) {
    const int d = bid;
    f32x4* prm = (f32x4*)smem;
    float* part = (float*)(smem + 2048);
    float* ktab = (float*)(smem + 2048 + 12288);
    if (tid < NS) {
      const float dt = rbf(expf(bf2f(ldt[d])));
      const float are = -rbf(expf(bf2f(lar[d * NS + tid])));
      const float ai = bf2f(aim[d * NS + tid]);
      const float cr = bf2f(cre[d * NS + tid]) * dt, ci = bf2f(cim[d * NS + tid]) * dt;
      const float mx = fmaxf(fabsf(cr), fabsf(ci));
      float lc = -1000.f, ph = 0.f;
      if (mx > 0.f) {
        const float a = cr / mx, b = ci / mx;
        lc = log2f(mx) + 0.5f * log2f(a * a + b * b);
        ph = atan2f(ci, cr) * 0.15915494309189535f;
      }
      f32x4 p = {are * dt * 1.4426950408889634f, ai * dt * 0.15915494309189535f, lc, ph};
      prm[tid] = p;
    }
    __syncthreads();
    const int i = lane & 15, kq = lane >> 4;
    const float A0 = (float)(128 * (i >> 3) + (i & 7));
    const float A1 = (float)(i < 8 ? 256 + 128 * (i >> 2) + 2 * (i & 3) : 512 + 128 * ((i - 8) >> 1) + 4 * (i & 1));
    const float A2 = (float)(i <= 8 ? 1024 + 128 * i : 0);
    const float Bp = (float)(8 * i);
    f32x4 acc0 = {}, acc1 = {}, acc2 = {};
#pragma unroll 2
    for (int t = 0; t < 8; ++t) {
      const f32x4 p = prm[32 * w + 4 * t + kq];
      float br, bi, ar, ai_;
      cexp_rev(0.f, p[0], 0.f, p[1], Bp, br, bi);
      cexp_rev(p[2], p[0], p[3], p[1], A0, ar, ai_);
      acc0 = __builtin_amdgcn_mfma_f32_16x16x4f32(ar, br, acc0, 0, 0, 0);
      acc0 = __builtin_amdgcn_mfma_f32_16x16x4f32(-ai_, bi, acc0, 0, 0, 0);
      cexp_rev(p[2], p[0], p[3], p[1], A1, ar, ai_);
      acc1 = __builtin_amdgcn_mfma_f32_16x16x4f32(ar, br, acc1, 0, 0, 0);
      acc1 = __builtin_amdgcn_mfma_f32_16x16x4f32(-ai_, bi, acc1, 0, 0, 0);
      cexp_rev(p[2], p[0], p[3], p[1], A2, ar, ai_);
      acc2 = __builtin_amdgcn_mfma_f32_16x16x4f32(ar, br, acc2, 0, 0, 0);
      acc2 = __builtin_amdgcn_mfma_f32_16x16x4f32(-ai_, bi, acc2, 0, 0, 0);
    }
#pragma unroll
    for (int r = 0; r < 4; ++r) {
      const int idx = (4 * kq + r) * 16 + i;
      part[(w * 3 + 0) * 256 + idx] = acc0[r];
      part[(w * 3 + 1) * 256 + idx] = acc1[r];
      part[(w * 3 + 2) * 256 + idx] = acc2[r];
    }
    __syncthreads();
#pragma unroll
    for (int s = 0; s < 3; ++s)
      ktab[s * 256 + tid] = 2.f * (part[(0 + s) * 256 + tid] + part[(3 + s) * 256 + tid] + part[(6 + s) * 256 + tid] +
                                   part[(9 + s) * 256 + tid]);
    __syncthreads();
    u16* rg = (u16*)(ws + WS_RG) + (size_t)d * RG_STRIDE;
    {
      const int q = tid;
      float v[8];
#pragma unroll
      for (int e = 0; e < 8; ++e) {
        const int l = 2047 - (8 * q + e);
        const int sh = l < 256 ? 0 : (l < 512 ? 1 : (l < 1024 ? 2 : 3));
        const int p = sh ? (((l + (1 << (sh - 1)) - 1 + ((l >> sh) & 1)) >> sh) << sh) : l;
        const int b = (p >> 3) & 15;
        int row;
        if (p < 256) row = 8 * (p >> 7) + (p & 7);
        else if (p < 512) row = 16 + 4 * ((p - 256) >> 7) + ((p & 7) >> 1);
        else if (p < 1024) row = 24 + 2 * ((p - 512) >> 7) + ((p & 7) >> 2);
        else row = 32 + ((p - 1024) >> 7);
        v[e] = ktab[row * 16 + b];
      }
      u32x4 o = {pk2(v[0], v[1]), pk2(v[2], v[3]), pk2(v[4], v[5]), pk2(v[6], v[7])};
      *(u32x4*)(rg + 8 * q) = o;
      if (tid < RG_STRIDE / 8 - 256) {
        u32x4 z = {0u, 0u, 0u, 0u};
        *(u32x4*)(rg + 2048 + 8 * tid) = z;
      }
    }
    return;
  }
  bid -= PREP_KGEN;
  if (bid < PREP_LN) {
    const int l0 = bid * 16;
    const int row = tid >> 4, t16 = tid & 15;
    const u16* src = xu + (size_t)(l0 + row) * D;
    u32x4 v[5];
#pragma unroll
    for (int i = 0; i < 5; ++i) v[i] = *(const u32x4*)(src + (t16 + 16 * i) * 8);
    float s = 0.f;
#pragma unroll
    for (int i = 0; i < 5; ++i) {
#pragma unroll
      for (int j = 0; j < 4; ++j) s += bflo(v[i][j]) + bfhi(v[i][j]);
      *(u32x4*)(smem + row * 1296 + (t16 + 16 * i) * 16) = v[i];
    }
    s += __shfl_xor(s, 1); s += __shfl_xor(s, 2); s += __shfl_xor(s, 4); s += __shfl_xor(s, 8);
    const float mean = s * (1.0f / D);
    float m2 = 0.f;
#pragma unroll
    for (int i = 0; i < 5; ++i)
#pragma unroll
      for (int j = 0; j < 4; ++j) {
        float a = bflo(v[i][j]) - mean, b = bfhi(v[i][j]) - mean;
        m2 += a * a + b * b;
      }
    m2 += __shfl_xor(m2, 1); m2 += __shfl_xor(m2, 2); m2 += __shfl_xor(m2, 4); m2 += __shfl_xor(m2, 8);
    if (t16 == 0) {
      f32x2 o = {s, m2};
      *(f32x2*)(ws + WS_ST0 + (size_t)(l0 + row) * 8) = o;
    }
    __syncthreads();
    u16* xt = (u16*)(ws + WS_XT0);
    const int g = lane >> 4, q = (lane & 15) >> 2, p = lane & 3, i16 = lane & 15;
#pragma unroll
    for (int it = 0; it < 5; ++it) {
      const int cb = (it * 4 + w) * 2 + (g >> 1);
      const char* base = smem + (8 * (g & 1) + q) * 1296 + cb * 32 + 8 * p;
      s16x4 lo = tr_read(base);
      s16x4 hi = tr_read(base + 4 * 1296);
      u32x2 a = __builtin_bit_cast(u32x2, lo), b = __builtin_bit_cast(u32x2, hi);
      u32x4 o = {a[0], a[1], b[0], b[1]};
      *(u32x4*)(xt + (size_t)(cb * 16 + i16) * L + l0 + 8 * (g & 1)) = o;
    }
    return;
  }
  bid -= PREP_LN;
  if (bid < PREP_WQ + PREP_WO) {
    const bool isq = bid < PREP_WQ;
    const int f = (isq ? bid : bid - PREP_WQ) * 4 + w;
    const int nt = f / 20, ks = f % 20;
    const int n = (lane & 15), k0 = 32 * ks + 8 * (lane >> 4);
    const u16* src;
    int ld, col;
    bool valid = true;
    if (!isq) { src = wo; ld = D; col = 16 * nt + n; }
    else if (nt < 40) { src = wq; ld = D; col = 16 * nt + n; }
    else if (nt < 42) { src = wk; ld = DH; col = 16 * (nt - 40) + n; valid = col < DH; }
    else { src = wv; ld = DH; col = 16 * (nt - 42) + n; valid = col < DH; }
    unsigned e[8];
#pragma unroll
    for (int j = 0; j < 8; ++j) e[j] = valid ? (unsigned)src[(size_t)(k0 + j) * ld + col] : 0u;
    u32x4 o = {e[0] | (e[1] << 16), e[2] | (e[3] << 16), e[4] | (e[5] << 16), e[6] | (e[7] << 16)};
    *(u32x4*)(ws + (isq ? WS_WQ : WS_WO) + (size_t)f * 1024 + lane * 16) = o;
    return;
  }
  bid -= PREP_WQ + PREP_WO;
  {
    for (int c = bid * 256 + tid; c < 64 * 256; c += PREP_KV * 256) {
      const int fr = (c >> 6) & 3, ln = c & 63;
      unsigned v = (fr >= 2 && (ln & 31) == DH) ? 0x3f803f80u : 0u;
      u32x4 o = {v, v, v, v};
      *(u32x4*)(ws + WS_KV + (size_t)c * 16) = o;
    }
  }
}

constexpr int CS = 4160;
template <int NP>
__global__ __launch_bounds__(256) void conv_kernel(const u16* __restrict__ xt, const f32x2* __restrict__ stats,
                                                   const u16* __restrict__ rg, const u16* __restrict__ dskip,
                                                   u16* __restrict__ xout) {
  __shared__ __attribute__((aligned(16))) char smem[8 * CS + 96 * 80];
  char* KC = smem;
  char* U = smem + 8 * CS;
  const int tid = threadIdx.x, lane = tid & 63, w = tid >> 6;
  const int d = blockIdx.x;
  const u32x4 xv = *(const u32x4*)(xt + (size_t)d * L + 8 * tid);
  const u16* rgd = rg + (size_t)d * RG_STRIDE;
  const u32x4 r0 = *(const u32x4*)(rgd + 8 * tid);
  const u32x4 r1 = *(const u32x4*)(rgd + 8 * tid + 8);
  const float dsk = bf2f(dskip[d]);
  float mb[8], sb[8];
#pragma unroll
  for (int e = 0; e < 8; ++e) {
    const int l = 8 * tid + e;
    float sum = 0.f, m2 = 0.f;
    if (NP == 1) {
      f32x2 s = stats[l];
      sum = s[0]; m2 = s[1];
    } else {
      f32x2 s[NP];
#pragma unroll
      for (int p = 0; p < NP; ++p) { s[p] = stats[l * NP + p]; sum += s[p][0]; }
      const float mean = sum * (1.0f / D);
#pragma unroll
      for (int p = 0; p < NP; ++p) {
        float dm = s[p][0] * ((float)NP / D) - mean;
        m2 += s[p][1] + dm * dm * ((float)D / NP);
      }
    }
    ln_params(sum, m2, mb[e], sb[e]);
  }
  {
    unsigned wv[8] = {r0[0], r0[1], r0[2], r0[3], r1[0], r1[1], r1[2], r1[3]};
#pragma unroll
    for (int s = 0; s < 8; ++s) {
      u32x4 o;
#pragma unroll
      for (int k = 0; k < 4; ++k) {
        if (s & 1) o[k] = __builtin_amdgcn_alignbit(wv[(s >> 1) + k + 1], wv[(s >> 1) + k], 16);
        else o[k] = wv[(s >> 1) + k];
      }
      *(u32x4*)(KC + s * CS + tid * 16) = o;
    }
    if (tid < 32) {
      u32x4 z = {0u, 0u, 0u, 0u};
      *(u32x4*)(KC + (tid >> 2) * CS + (256 + (tid & 3)) * 16) = z;
    }
  }
  float xf[8], uf[8];
#pragma unroll
  for (int j = 0; j < 4; ++j) { xf[2 * j] = bflo(xv[j]); xf[2 * j + 1] = bfhi(xv[j]); }
#pragma unroll
  for (int e = 0; e < 8; ++e) uf[e] = rbf(rbf(xf[e] - mb[e]) * sb[e]);
  {
    u32x4 o = {pk2(uf[0], uf[1]), pk2(uf[2], uf[3]), pk2(uf[4], uf[5]), pk2(uf[6], uf[7])};
    *(u32x4*)(U + ((tid >> 2) + 32) * 80 + (tid & 3) * 16) = o;
    if (tid < 128) {
      u32x4 z = {0u, 0u, 0u, 0u};
      *(u32x4*)(U + (tid >> 2) * 80 + (tid & 3) * 16) = z;
    }
  }
  __syncthreads();
  const int b = lane & 31, h = lane >> 5;
  const char* kbase = KC + (7 - (b & 7)) * CS + 16 * (255 - (b >> 3) + h);
  const char* ubase = U + (b + 32) * 80 + 16 * h;
  f32x16 acc0 = {}, acc1 = {};
  int c0, c1, c2;
  if (w == 0) { c0 = 0; c1 = 12; c2 = 12; }
  else if (w == 1) { c0 = 12; c1 = 24; c2 = 24; }
  else if (w == 2) { c0 = 24; c1 = 32; c2 = 40; }
  else { c0 = 40; c1 = 40; c2 = 64; }
  for (int c = c0; c < c1; ++c) {
#pragma unroll
    for (int ks = 0; ks < 2; ++ks) {
      const bf16x8 kf = *(const bf16x8*)(kbase - 64 * c + 32 * ks);
      const bf16x8 u0 = *(const bf16x8*)(ubase - 80 * c + 32 * ks);
      const bf16x8 u1 = *(const bf16x8*)(ubase + 80 * (32 - c) + 32 * ks);
      acc0 = __builtin_amdgcn_mfma_f32_32x32x16_bf16(u0, kf, acc0, 0, 0, 0);
      acc1 = __builtin_amdgcn_mfma_f32_32x32x16_bf16(u1, kf, acc1, 0, 0, 0);
    }
  }
  for (int c = c1; c < c2; ++c) {
#pragma unroll
    for (int ks = 0; ks < 2; ++ks) {
      const bf16x8 kf = *(const bf16x8*)(kbase - 64 * c + 32 * ks);
      const bf16x8 u1 = *(const bf16x8*)(ubase + 80 * (32 - c) + 32 * ks);
      acc1 = __builtin_amdgcn_mfma_f32_32x32x16_bf16(u1, kf, acc1, 0, 0, 0);
    }
  }
  __syncthreads();
  float* part = (float*)smem;
#pragma unroll
  for (int r = 0; r < 16; ++r) {
    const int a = crow(r, h);
    part[w * 2048 + 32 * a + b] = acc0[r];
    part[w * 2048 + 1024 + 32 * a + b] = acc1[r];
  }
  __syncthreads();
  float y[8];
#pragma unroll
  for (int e = 0; e < 8; ++e) y[e] = 0.f;
#pragma unroll
  for (int ww = 0; ww < 4; ++ww) {
    const f32x4 p0 = *(const f32x4*)(part + ww * 2048 + 8 * tid);
    const f32x4 p1 = *(const f32x4*)(part + ww * 2048 + 8 * tid + 4);
#pragma unroll
    for (int e = 0; e < 4; ++e) { y[e] += p0[e]; y[4 + e] += p1[e]; }
  }
  float o[8];
#pragma unroll
  for (int e = 0; e < 8; ++e) o[e] = xf[e] + rbf(rbf(y[e]) + rbf(uf[e] * dsk));
  u32x4 ov = {pk2(o[0], o[1]), pk2(o[2], o[3]), pk2(o[4], o[5]), pk2(o[6], o[7])};
  *(u32x4*)(xout + (size_t)d * L + 8 * tid) = ov;
}

__global__ __launch_bounds__(512, 2) void qkv_kernel(const u16* __restrict__ xt, const char* __restrict__ wf,
                                                     u16* __restrict__ Q, char* __restrict__ kvf) {
  __shared__ __attribute__((aligned(16))) char smem[640 * 64 + 2048];
  f32x4* red = (f32x4*)(smem + 640 * 64);
  const int tid = threadIdx.x, lane = tid & 63, w = tid >> 6;
  const int idx = blockIdx.x >> 3, grp = (idx >> 3) * 8 + (blockIdx.x & 7), mem = idx & 7;
  const int rt = grp * 2 + (mem >> 2), ns = mem & 3;
  const int l0 = 32 * rt;
  const int lp = lane & 15, dsub = lane >> 4;
  unsigned v[20];
  const u16* xb = xt + (size_t)(4 * w + dsub) * L + l0 + 2 * lp;
#pragma unroll
  for (int i = 0; i < 20; ++i) v[i] = *(const unsigned*)(xb + (size_t)32 * i * L);
  const unsigned cshift = *(const unsigned*)(xt + l0 + 2 * lp);
  const int nt0 = ns * 11 + w;
  const int ncnt = (w < 3) ? 2 : 1;
  const char* wbase = wf + (size_t)nt0 * 20 * 1024 + lane * 16;
  bf16x8 w0[20], w1[20];
#pragma unroll
  for (int ks = 0; ks < 20; ++ks) w0[ks] = *(const bf16x8*)(wbase + ks * 1024);
  if (ncnt == 2) {
#pragma unroll
    for (int ks = 0; ks < 20; ++ks) w1[ks] = *(const bf16x8*)(wbase + 8 * 20 * 1024 + ks * 1024);
  } else {
#pragma unroll
    for (int ks = 0; ks < 20; ++ks) w1[ks] = bf16x8{};
  }
  const float c0 = bflo(cshift), c1 = bfhi(cshift);
  f32x4 st = {0.f, 0.f, 0.f, 0.f};
#pragma unroll
  for (int i = 0; i < 20; ++i) {
    const float a = bflo(v[i]) - c0, b = bfhi(v[i]) - c1;
    st[0] += a; st[1] = __builtin_fmaf(a, a, st[1]);
    st[2] += b; st[3] = __builtin_fmaf(b, b, st[3]);
  }
#pragma unroll
  for (int e = 0; e < 4; ++e) { st[e] += __shfl_xor(st[e], 16); st[e] += __shfl_xor(st[e], 32); }
  if (lane < 16) red[w * 16 + lp] = st;
  __syncthreads();
  {
    f32x4 t = red[lp];
#pragma unroll
    for (int ww = 1; ww < 8; ++ww) t += red[ww * 16 + lp];
    st = t;
  }
  float mb0, sb0, mb1, sb1;
  ln_params(__builtin_fmaf(c0, (float)D, st[0]), fmaxf(st[1] - st[0] * st[0] * (1.0f / D), 0.f), mb0, sb0);
  ln_params(__builtin_fmaf(c1, (float)D, st[2]), fmaxf(st[3] - st[2] * st[2] * (1.0f / D), 0.f), mb1, sb1);
#pragma unroll
  for (int i = 0; i < 20; ++i) {
    const int d = 32 * i + 4 * w + dsub;
    const unsigned t = pk2(bflo(v[i]) - mb0, bfhi(v[i]) - mb1);
    const unsigned o = pk2(bflo(t) * sb0, bfhi(t) * sb1);
    *(unsigned*)(smem + d * 64 + ((4 * lp) ^ (((d >> 3) & 1) << 5))) = o;
  }
  __syncthreads();
  const int g = lane >> 4, q = (lane & 15) >> 2, p = lane & 3;
  const char* ub0 = smem + (8 * g + q) * 64 + 32 * (0 ^ (g & 1)) + 8 * p;
  const char* ub1 = smem + (8 * g + q) * 64 + 32 * (1 ^ (g & 1)) + 8 * p;
  f32x4 acc[2][2];
#pragma unroll
  for (int a = 0; a < 2; ++a) { acc[a][0] = f32x4{}; acc[a][1] = f32x4{}; }
#pragma unroll
  for (int ks = 0; ks < 20; ++ks) {
    s16x4 a0 = tr_read(ub0 + ks * 2048), a1 = tr_read(ub0 + ks * 2048 + 256);
    s16x4 b0 = tr_read(ub1 + ks * 2048), b1 = tr_read(ub1 + ks * 2048 + 256);
    bf16x8 u0 = __builtin_bit_cast(bf16x8, __builtin_shufflevector(a0, a1, 0, 1, 2, 3, 4, 5, 6, 7));
    bf16x8 u1 = __builtin_bit_cast(bf16x8, __builtin_shufflevector(b0, b1, 0, 1, 2, 3, 4, 5, 6, 7));
    acc[0][0] = __builtin_amdgcn_mfma_f32_16x16x32_bf16(w0[ks], u0, acc[0][0], 0, 0, 0);
    acc[0][1] = __builtin_amdgcn_mfma_f32_16x16x32_bf16(w0[ks], u1, acc[0][1], 0, 0, 0);
    acc[1][0] = __builtin_amdgcn_mfma_f32_16x16x32_bf16(w1[ks], u0, acc[1][0], 0, 0, 0);
    acc[1][1] = __builtin_amdgcn_mfma_f32_16x16x32_bf16(w1[ks], u1, acc[1][1], 0, 0, 0);
  }
  const float qscale = 0.22360679774997896f * 1.4426950408889634f;
#pragma unroll
  for (int a = 0; a < 2; ++a) {
    if (a >= ncnt) break;
    const int nt = nt0 + 8 * a;
#pragma unroll
    for (int lt = 0; lt < 2; ++lt) {
      const int l = l0 + 16 * lt + (lane & 15);
      const f32x4 r = acc[a][lt];
      if (nt < 40) {
        u32x2 o = {pk2(r[0] * qscale, r[1] * qscale), pk2(r[2] * qscale, r[3] * qscale)};
        *(u32x2*)(Q + (size_t)l * D + 16 * nt + 4 * g) = o;
      } else if (nt < 42) {
        const int r32 = l & 31;
        u32x2 o = {pk2(r[0], r[1]), pk2(r[2], r[3])};
        *(u32x2*)(kvf + (size_t)rt * 4096 + (nt - 40) * 1024 + (r32 + 32 * (g >> 1)) * 16 + 8 * (g & 1)) = o;
      } else {
        const int r32 = l & 31;
        const int s_ = r32 >> 4, jh = (r32 >> 3) & 1, hh = (r32 >> 2) & 1, jl = r32 & 3;
        const int j = 4 * jh + jl;
#pragma unroll
        for (int e = 0; e < 4; ++e) {
          const int dv = 16 * (nt - 42) + 4 * g + e;
          if (dv < DH) {
            u16 bv = (u16)(pk2(r[e], 0.f) & 0xffffu);
            *(u16*)(kvf + (size_t)rt * 4096 + (2 + s_) * 1024 + (dv + 32 * hh) * 16 + 2 * j) = bv;
          }
        }
      }
    }
  }
}

constexpr int ACH = 16;
DI void attn_pack(const f32x16& s, float delta, u32x4& pa, u32x4& pb) {
#pragma unroll
  for (int r = 0; r < 4; ++r) {
    pa[r] = pk2(__builtin_amdgcn_exp2f(s[2 * r] - delta), __builtin_amdgcn_exp2f(s[2 * r + 1] - delta));
    pb[r] = pk2(__builtin_amdgcn_exp2f(s[8 + 2 * r] - delta), __builtin_amdgcn_exp2f(s[8 + 2 * r + 1] - delta));
  }
}
DI void attn_safe_tile(const char* tb, const bf16x8& qf0, const bf16x8& qf1, f32x16& o, f32x16& negm, bool first) {
  const bf16x8 k0 = *(const bf16x8*)(tb);
  const bf16x8 k1 = *(const bf16x8*)(tb + 1024);
  const bf16x8 v0 = *(const bf16x8*)(tb + 2048);
  const bf16x8 v1 = *(const bf16x8*)(tb + 3072);
  f32x16 s = __builtin_amdgcn_mfma_f32_32x32x16_bf16(k0, qf0, negm, 0, 0, 0);
  s = __builtin_amdgcn_mfma_f32_32x32x16_bf16(k1, qf1, s, 0, 0, 0);
  float tm = s[0];
#pragma unroll
  for (int r = 1; r < 16; ++r) tm = fmaxf(tm, s[r]);
  tm = fmaxf(tm, __shfl_xor(tm, 32));
  const float delta = first ? tm : fmaxf(tm, 0.f);
  const float sc = first ? 0.f : __builtin_amdgcn_exp2f(-delta);
#pragma unroll
  for (int r = 0; r < 16; ++r) { o[r] *= sc; negm[r] -= delta; }
  u32x4 pa, pb;
  attn_pack(s, delta, pa, pb);
  o = __builtin_amdgcn_mfma_f32_32x32x16_bf16(v0, __builtin_bit_cast(bf16x8, pa), o, 0, 0, 0);
  o = __builtin_amdgcn_mfma_f32_32x32x16_bf16(v1, __builtin_bit_cast(bf16x8, pb), o, 0, 0, 0);
}
#define SBAR() __builtin_amdgcn_sched_barrier(0)
#define EXP4(P, I, S, B)                                                                   \
  P[I] = pk2(__builtin_amdgcn_exp2f(S[B]), __builtin_amdgcn_exp2f(S[B + 1]));              \
  P[I + 1] = pk2(__builtin_amdgcn_exp2f(S[B + 2]), __builtin_amdgcn_exp2f(S[B + 3]));
template <int T0>
DI void attn_fast_chunk(const char* base, const bf16x8& qf0, const bf16x8& qf1, f32x16& o, const f32x16& negm) {
  f32x16 s_cur, s_nxt;
  bf16x8 k0, k1, v0, v1, v1p, kn0, kn1, vn0, vn1;
  u32x4 pa, pb, pbp;
  {
    k0 = *(const bf16x8*)(base + T0 * 4096);
    k1 = *(const bf16x8*)(base + T0 * 4096 + 1024);
    v0 = *(const bf16x8*)(base + T0 * 4096 + 2048);
    v1 = *(const bf16x8*)(base + T0 * 4096 + 3072);
    s_cur = __builtin_amdgcn_mfma_f32_32x32x16_bf16(k0, qf0, negm, 0, 0, 0);
    s_cur = __builtin_amdgcn_mfma_f32_32x32x16_bf16(k1, qf1, s_cur, 0, 0, 0);
    k0 = *(const bf16x8*)(base + (T0 + 1) * 4096);
    k1 = *(const bf16x8*)(base + (T0 + 1) * 4096 + 1024);
  }
#pragma unroll
  for (int t = T0; t < ACH; ++t) {
    SBAR();
    if (t + 1 < ACH) s_nxt = __builtin_amdgcn_mfma_f32_32x32x16_bf16(k0, qf0, negm, 0, 0, 0);
    if (t + 2 < ACH) {
      kn0 = *(const bf16x8*)(base + (t + 2) * 4096);
      kn1 = *(const bf16x8*)(base + (t + 2) * 4096 + 1024);
    }
    if (t + 1 < ACH) {
      vn0 = *(const bf16x8*)(base + (t + 1) * 4096 + 2048);
      vn1 = *(const bf16x8*)(base + (t + 1) * 4096 + 3072);
    }
    SBAR();
    EXP4(pa, 0, s_cur, 0);
    SBAR();
    if (t > T0) o = __builtin_amdgcn_mfma_f32_32x32x16_bf16(v1p, __builtin_bit_cast(bf16x8, pbp), o, 0, 0, 0);
    SBAR();
    EXP4(pa, 2, s_cur, 4);
    SBAR();
    if (t + 1 < ACH) s_nxt = __builtin_amdgcn_mfma_f32_32x32x16_bf16(k1, qf1, s_nxt, 0, 0, 0);
    SBAR();
    EXP4(pb, 0, s_cur, 8);
    SBAR();
    o = __builtin_amdgcn_mfma_f32_32x32x16_bf16(v0, __builtin_bit_cast(bf16x8, pa), o, 0, 0, 0);
    SBAR();
    EXP4(pb, 2, s_cur, 12);
    pbp = pb; v1p = v1;
    s_cur = s_nxt; k0 = kn0; k1 = kn1; v0 = vn0; v1 = vn1;
  }
  SBAR();
  o = __builtin_amdgcn_mfma_f32_32x32x16_bf16(v1p, __builtin_bit_cast(bf16x8, pbp), o, 0, 0, 0);
  SBAR();
}

__global__ __launch_bounds__(512, 2) void attn_kernel(const u16* __restrict__ Q, const char* __restrict__ kvf,
                                                      u16* __restrict__ O) {
  __shared__ __attribute__((aligned(16))) char smem[2 * ACH * 4096];
  const int tid = threadIdx.x, lane = tid & 63, w = tid >> 6;
  const int hq = lane & 31, h = lane >> 5;
  const int row = blockIdx.x * 8 + w;
  auto stage = [&](int chunk, int buf) {
#pragma unroll
    for (int i = 0; i < ACH / 2; ++i) {
      const char* src = kvf + (size_t)chunk * ACH * 4096 + w * (ACH * 512) + i * 1024 + lane * 16;
      __builtin_amdgcn_global_load_lds((const __attribute__((address_space(1))) void*)src,
                                       (__attribute__((address_space(3))) void*)(smem + buf * ACH * 4096 + w * (ACH * 512) + i * 1024),
                                       16, 0, 0);
    }
  };
  stage(0, 0);
  bf16x8 qf0, qf1;
  {
    const u16* qp = Q + (size_t)row * D + hq * DH;
    u32x2 a = *(const u32x2*)(qp + 8 * h), b = *(const u32x2*)(qp + 8 * h + 4);
    u32x4 t = {a[0], a[1], b[0], b[1]};
    qf0 = __builtin_bit_cast(bf16x8, t);
    u32x2 c = *(const u32x2*)(qp + 16);
    u32x4 t1 = {h ? 0u : c[0], h ? 0u : c[1], 0u, 0u};
    qf1 = __builtin_bit_cast(bf16x8, t1);
  }
  f32x16 o = {};
  f32x16 negm = {};
  __syncthreads();
  for (int ch = 0; ch < 64 / ACH; ++ch) {
    if (ch + 1 < 64 / ACH) stage(ch + 1, (ch + 1) & 1);
    const char* base = smem + (ch & 1) * ACH * 4096 + lane * 16;
    f32x16 ock;
    if (ch == 0) {
      attn_safe_tile(base, qf0, qf1, o, negm, true);
      ock = o;
      attn_fast_chunk<1>(base, qf0, qf1, o, negm);
    } else {
      ock = o;
      attn_fast_chunk<0>(base, qf0, qf1, o, negm);
    }
    const float lsum = h ? o[8] : 0.f;
    if (__builtin_expect(__any(!(lsum < 65536.f)), 0)) {
      if (__any(!(lsum < 1.8446744e19f))) {
        o = ock;
        for (int t = (ch == 0 ? 1 : 0); t < ACH; ++t) attn_safe_tile(base + t * 4096, qf0, qf1, o, negm, false);
      }
      float ls = __shfl(o[8], hq + 32);
      int e = (int)((__float_as_uint(ls) >> 23) & 0xff) - 127;
      e = (ls > 1.f && e < 128) ? e : 0;
      const float sc = __uint_as_float((unsigned)(127 - e) << 23);
      const float fe = (float)e;
#pragma unroll
      for (int r = 0; r < 16; ++r) { o[r] *= sc; negm[r] -= fe; }
    }
    __syncthreads();
  }
  float lsum = o[8];
  lsum = __shfl(lsum, hq + 32);
  const float inv = 1.0f / lsum;
#pragma unroll
  for (int g = 0; g < 3; ++g) {
    const int dv = 8 * g + 4 * h;
    if (dv < DH) {
      const int k = hq * DH + dv;
      u32x2 ov = {pk2(o[4 * g] * inv, o[4 * g + 1] * inv), pk2(o[4 * g + 2] * inv, o[4 * g + 3] * inv)};
      const size_t off = ((size_t)(((row >> 4) * 20 + (k >> 5)) * 64 + ((k >> 3) & 3) * 16 + (row & 15))) * 8 + (k & 7);
      *(u32x2*)(O + off) = ov;
    }
  }
}

template <bool FINAL>
__global__ __launch_bounds__(320, 2) void oproj_kernel(const u16* __restrict__ O, const char* __restrict__ wf,
                                                       const u16* __restrict__ xres, u16* __restrict__ xout,
                                                       f32x2* __restrict__ stats) {
  __shared__ __attribute__((aligned(16))) char smem[40 * 1024];
  const int tid = threadIdx.x, lane = tid & 63, w = tid >> 6;
  const int idx = blockIdx.x >> 3, grp = (idx >> 2) * 8 + (blockIdx.x & 7);
  const int rt = grp, ns = idx & 3;
  const int l0 = 32 * rt;
#pragma unroll
  for (int i = 0; i < 8; ++i) {
    const char* src = (const char*)O + (size_t)rt * 40960 + (w * 8 + i) * 1024 + lane * 16;
    __builtin_amdgcn_global_load_lds((const __attribute__((address_space(1))) void*)src,
                                     (__attribute__((address_space(3))) void*)(smem + (w * 8 + i) * 1024), 16, 0, 0);
  }
  const int g = lane >> 4, i16 = lane & 15;
  const int nt0 = ns * 10 + 2 * w;
  const char* wbase = wf + (size_t)nt0 * 20 * 1024 + lane * 16;
  bf16x8 w0[20], w1[20];
#pragma unroll
  for (int ks = 0; ks < 20; ++ks) w0[ks] = *(const bf16x8*)(wbase + ks * 1024);
#pragma unroll
  for (int ks = 0; ks < 20; ++ks) w1[ks] = *(const bf16x8*)(wbase + 20 * 1024 + ks * 1024);
  u32x2 xr[2][2];
  u16 xs[2][2][4];
#pragma unroll
  for (int a = 0; a < 2; ++a)
#pragma unroll
    for (int lt = 0; lt < 2; ++lt) {
      if (FINAL) {
#pragma unroll
        for (int e = 0; e < 4; ++e) xs[a][lt][e] = xres[(size_t)(16 * (nt0 + a) + 4 * g + e) * L + l0 + 16 * lt + i16];
      } else {
        xr[a][lt] = *(const u32x2*)(xres + (size_t)(16 * (nt0 + a) + i16) * L + l0 + 16 * lt + 4 * g);
      }
    }
  __syncthreads();
  const char* obase = smem + lane * 16;
  f32x4 acc[2][2];
  acc[0][0] = f32x4{}; acc[0][1] = f32x4{}; acc[1][0] = f32x4{}; acc[1][1] = f32x4{};
#pragma unroll
  for (int ks = 0; ks < 20; ++ks) {
    const bf16x8 o0 = *(const bf16x8*)(obase + ks * 1024);
    const bf16x8 o1 = *(const bf16x8*)(obase + 20 * 1024 + ks * 1024);
    if (FINAL) {
      acc[0][0] = __builtin_amdgcn_mfma_f32_16x16x32_bf16(w0[ks], o0, acc[0][0], 0, 0, 0);
      acc[0][1] = __builtin_amdgcn_mfma_f32_16x16x32_bf16(w0[ks], o1, acc[0][1], 0, 0, 0);
      acc[1][0] = __builtin_amdgcn_mfma_f32_16x16x32_bf16(w1[ks], o0, acc[1][0], 0, 0, 0);
      acc[1][1] = __builtin_amdgcn_mfma_f32_16x16x32_bf16(w1[ks], o1, acc[1][1], 0, 0, 0);
    } else {
      acc[0][0] = __builtin_amdgcn_mfma_f32_16x16x32_bf16(o0, w0[ks], acc[0][0], 0, 0, 0);
      acc[0][1] = __builtin_amdgcn_mfma_f32_16x16x32_bf16(o1, w0[ks], acc[0][1], 0, 0, 0);
      acc[1][0] = __builtin_amdgcn_mfma_f32_16x16x32_bf16(o0, w1[ks], acc[1][0], 0, 0, 0);
      acc[1][1] = __builtin_amdgcn_mfma_f32_16x16x32_bf16(o1, w1[ks], acc[1][1], 0, 0, 0);
    }
  }
  if (FINAL) {
#pragma unroll
    for (int a = 0; a < 2; ++a)
#pragma unroll
      for (int lt = 0; lt < 2; ++lt) {
        const int l = l0 + 16 * lt + i16;
        const int n = 16 * (nt0 + a) + 4 * g;
        float r[4];
#pragma unroll
        for (int e = 0; e < 4; ++e) r[e] = bf2f(xs[a][lt][e]) + rbf(acc[a][lt][e]);
        u32x2 ov = {pk2(r[0], r[1]), pk2(r[2], r[3])};
        *(u32x2*)(xout + (size_t)l * D + n) = ov;
      }
  } else {
    __syncthreads();
    float* st = (float*)smem;
#pragma unroll
    for (int a = 0; a < 2; ++a)
#pragma unroll
      for (int lt = 0; lt < 2; ++lt) {
        const int n = 16 * (nt0 + a) + i16;
        const int l = l0 + 16 * lt + 4 * g;
        float r[4];
        r[0] = rbf(bflo(xr[a][lt][0]) + rbf(acc[a][lt][0]));
        r[1] = rbf(bfhi(xr[a][lt][0]) + rbf(acc[a][lt][1]));
        r[2] = rbf(bflo(xr[a][lt][1]) + rbf(acc[a][lt][2]));
        r[3] = rbf(bfhi(xr[a][lt][1]) + rbf(acc[a][lt][3]));
        u32x2 ov = {pk2(r[0], r[1]), pk2(r[2], r[3])};
        *(u32x2*)(xout + (size_t)n * L + l) = ov;
        const int nl = 16 * (2 * w + a) + i16;
#pragma unroll
        for (int e = 0; e < 4; ++e) st[(16 * lt + 4 * g + e) * 164 + nl] = r[e];
      }
    __syncthreads();
    if (tid < 256) {
      const int l = tid >> 3, p8 = tid & 7;
      float v[20];
      float s = 0.f;
#pragma unroll
      for (int e = 0; e < 20; ++e) { v[e] = st[l * 164 + p8 + 8 * e]; s += v[e]; }
      s += __shfl_xor(s, 1); s += __shfl_xor(s, 2); s += __shfl_xor(s, 4);
      const float mean = s * (1.0f / 160.f);
      float m2 = 0.f;
#pragma unroll
      for (int e = 0; e < 20; ++e) { float a = v[e] - mean; m2 += a * a; }
      m2 += __shfl_xor(m2, 1); m2 += __shfl_xor(m2, 2); m2 += __shfl_xor(m2, 4);
      if (p8 == 0) {
        f32x2 o = {s, m2};
        stats[(size_t)(l0 + l) * 4 + ns] = o;
      }
    }
  }
}

extern "C" void kernel_launch(void* const* d_in, const int* in_sizes, int n_in, void* d_out, int out_size,
                              void* d_ws, size_t ws_size, hipStream_t stream) {
  const u16* xu = (const u16*)d_in[0];
  const u16* lar = (const u16*)d_in[1];
  const u16* aim = (const u16*)d_in[2];
  const u16* cre = (const u16*)d_in[3];
  const u16* cim = (const u16*)d_in[4];
  const u16* ldt = (const u16*)d_in[5];
  const u16* dsk = (const u16*)d_in[6];
  const u16* wq = (const u16*)d_in[8];
  const u16* wk = (const u16*)d_in[9];
  const u16* wv = (const u16*)d_in[10];
  const u16* wo = (const u16*)d_in[11];
  char* ws = (char*)d_ws;
  u16* out = (u16*)d_out;
  const u16* rg = (const u16*)(ws + WS_RG);
  u16* xt0 = (u16*)(ws + WS_XT0);
  u16* xt1 = (u16*)(ws + WS_XT1);
  u16* xt2 = (u16*)(ws + WS_XT2);
  u16* xt3 = (u16*)(ws + WS_XT3);
  f32x2* st0 = (f32x2*)(ws + WS_ST0);
  f32x2* st2 = (f32x2*)(ws + WS_ST2);
  u16* Q = (u16*)(ws + WS_Q);
  u16* O = (u16*)(ws + WS_O);
  char* kvf = ws + WS_KV;

  prep_kernel<<<PREP_BLOCKS, 256, 0, stream>>>(xu, lar, aim, cre, cim, ldt, wq, wk, wv, wo, ws);
  conv_kernel<1><<<D, 256, 0, stream>>>(xt0, st0, rg, dsk, xt1);
  qkv_kernel<<<256, 512, 0, stream>>>(xt1, ws + WS_WQ, Q, kvf);
  attn_kernel<<<256, 512, 0, stream>>>(Q, kvf, O);
  oproj_kernel<false><<<256, 320, 0, stream>>>(O, ws + WS_WO, xt1, xt2, st2);
  conv_kernel<4><<<D, 256, 0, stream>>>(xt2, st2, rg, dsk, xt3);
  qkv_kernel<<<256, 512, 0, stream>>>(xt3, ws + WS_WQ, Q, kvf);
  attn_kernel<<<256, 512, 0, stream>>>(Q, kvf, O);
  oproj_kernel<true><<<256, 320, 0, stream>>>(O, ws + WS_WO, xt3, out, nullptr);
}
```

```cpp
#include <hip/hip_runtime.h>
#include <hip/hip_bf16.h>
#include <stdint.h>

typedef __attribute__((ext_vector_type(8))) __bf16 bf16x8;
typedef __attribute__((ext_vector_type(2))) __bf16 bf16x2;
typedef __attribute__((ext_vector_type(4))) short s16x4;
typedef __attribute__((ext_vector_type(16))) float f32x16;
typedef __attribute__((ext_vector_type(4))) float f32x4;
typedef __attribute__((ext_vector_type(2))) float f32x2;
typedef __attribute__((ext_vector_type(4))) unsigned u32x4;
typedef __attribute__((ext_vector_type(2))) unsigned u32x2;
typedef unsigned short u16;
#define DI __device__ __forceinline__

constexpr int L = 2048, D = 640, NS = 128, H = 32, DH = 20;
constexpr int RG_STRIDE = 2112;

constexpr int LT = 2112;
constexpr size_t SZ_XT = (size_t)D * LT * 2;
constexpr size_t WS_RG = 0;
constexpr size_t WS_XT0 = WS_RG + (size_t)D * RG_STRIDE * 2;
constexpr size_t WS_XT1 = WS_XT0 + SZ_XT;
constexpr size_t WS_XT2 = WS_XT1 + SZ_XT;
constexpr size_t WS_XT3 = WS_XT2 + SZ_XT;
constexpr size_t WS_ST0 = WS_XT3 + SZ_XT;
constexpr size_t WS_ST2 = WS_ST0 + (size_t)L * 8;
constexpr size_t WS_SF2 = WS_ST2 + (size_t)L * 4 * 8;
constexpr size_t WS_CNT = WS_SF2 + (size_t)L * 8;
constexpr size_t WS_WQ = WS_CNT + (size_t)3 * 64 * 128;
constexpr size_t WS_WO = WS_WQ + (size_t)44 * 20 * 1024;
constexpr size_t WS_Q = WS_WO + (size_t)40 * 20 * 1024;
constexpr size_t WS_KV = WS_Q + SZ_XT;
constexpr size_t WS_O = WS_KV + (size_t)64 * 4096;
constexpr size_t WS_END = WS_O + SZ_XT;

#define STAMP(kid, i) do {} while (0)
DI float bf2f(u16 v) { return __uint_as_float(((unsigned)v) << 16); }
DI float bflo(unsigned w) { return __uint_as_float(w << 16); }
DI float bfhi(unsigned w) { return __uint_as_float(w & 0xffff0000u); }
DI unsigned pk2(float a, float b) {
  f32x2 f = {a, b};
  bf16x2 h = __builtin_convertvector(f, bf16x2);
  return __builtin_bit_cast(unsigned, h);
}
DI float rbf(float a) {
  return __uint_as_float(pk2(a, 0.f) << 16);
}
DI int crow(int reg, int h) { return (reg & 3) + 8 * (reg >> 2) + 4 * h; }
typedef __attribute__((address_space(3))) s16x4 lds_s16x4;
DI s16x4 tr_read(const char* p) { return __builtin_amdgcn_ds_read_tr16_b64_v4i16((lds_s16x4*)p); }

DI void cexpf_(float re, float im, float p, float& er, float& ei) {
  float ar = re * p, ai = im * p;
  float mag = __builtin_amdgcn_exp2f(ar * 1.4426950408889634f);
  float n = __builtin_rintf(ai * 0.15915494309189535f);
  float r = __builtin_fmaf(-n, 6.2831854820251465f, ai);
  r = __builtin_fmaf(-n, -1.7484555314695172e-07f, r);
  float rev = r * 0.15915494309189535f;
  er = mag * __builtin_amdgcn_cosf(rev);
  ei = mag * __builtin_amdgcn_sinf(rev);
}

DI void ln_params(float sum, float m2, float& mb, float& sb) {
  mb = rbf(sum * (1.0f / D));
  float vb = rbf(m2 * (1.0f / D));
  sb = __builtin_amdgcn_rcpf(rbf(__builtin_amdgcn_sqrtf(rbf(vb + 1e-5f))));
}

constexpr int PREP_KGEN = 640, PREP_LN = 128, PREP_WQ = 100, PREP_WO = 100, PREP_WKV = 20, PREP_KV = 16;
constexpr int PREP_BLOCKS = PREP_KGEN + PREP_LN + PREP_WQ + PREP_WO + PREP_WKV + PREP_KV;

DI void cexp_rev(float e0, float e1, float f0, float f1, float p, float& er, float& ei) {
  const float mag = __builtin_amdgcn_exp2f(__builtin_fmaf(e1, p, e0));
  const float rev = __builtin_amdgcn_fractf(__builtin_fmaf(f1, p, f0));
  er = mag * __builtin_amdgcn_cosf(rev);
  ei = mag * __builtin_amdgcn_sinf(rev);
}

__global__ __launch_bounds__(256) void prep_kernel(const u16* __restrict__ xu, const u16* __restrict__ lar,
                                                   const u16* __restrict__ aim, const u16* __restrict__ cre,
                                                   const u16* __restrict__ cim, const u16* __restrict__ ldt,
                                                   const u16* __restrict__ wq, const u16* __restrict__ wk,
                                                   const u16* __restrict__ wv, const u16* __restrict__ wo,
                                                   char* __restrict__ ws) {
  __shared__ __attribute__((aligned(16))) char smem[16 * 1296 + 64];
  const int tid = threadIdx.x, lane = tid & 63, w = tid >> 6;
  int bid = blockIdx.x;
  if (bid < PREP_KGEN) {
    const int d = bid;
    f32x4* prm = (f32x4*)smem;
    float* part = (float*)(smem + 2048);
    float* ktab = (float*)(smem + 2048 + 12288);
    if (tid < NS) {
      const float dt = rbf(expf(bf2f(ldt[d])));
      const float are = -rbf(expf(bf2f(lar[d * NS + tid])));
      const float ai = bf2f(aim[d * NS + tid]);
      const float cr = bf2f(cre[d * NS + tid]) * dt, ci = bf2f(cim[d * NS + tid]) * dt;
      const float mx = fmaxf(fabsf(cr), fabsf(ci));
      float lc = -1000.f, ph = 0.f;
      if (mx > 0.f) {
        const float a = cr / mx, b = ci / mx;
        lc = log2f(mx) + 0.5f * log2f(a * a + b * b);
        ph = atan2f(ci, cr) * 0.15915494309189535f;
      }
      f32x4 p = {are * dt * 1.4426950408889634f, ai * dt * 0.15915494309189535f, lc, ph};
      prm[tid] = p;
    }
    __syncthreads();
    const int i = lane & 15, kq = lane >> 4;
    const float A0 = (float)(128 * (i >> 3) + (i & 7));
    const float A1 = (float)(i < 8 ? 256 + 128 * (i >> 2) + 2 * (i & 3) : 512 + 128 * ((i - 8) >> 1) + 4 * (i & 1));
    const float A2 = (float)(i <= 8 ? 1024 + 128 * i : 0);
    const float Bp = (float)(8 * i);
    f32x4 acc0 = {}, acc1 = {}, acc2 = {};
#pragma unroll 2
    for (int t = 0; t < 8; ++t) {
      const f32x4 p = prm[32 * w + 4 * t + kq];
      float br, bi, ar, ai_;
      cexp_rev(0.f, p[0], 0.f, p[1], Bp, br, bi);
      cexp_rev(p[2], p[0], p[3], p[1], A0, ar, ai_);
      acc0 = __builtin_amdgcn_mfma_f32_16x16x4f32(ar, br, acc0, 0, 0, 0);
      acc0 = __builtin_amdgcn_mfma_f32_16x16x4f32(-ai_, bi, acc0, 0, 0, 0);
      cexp_rev(p[2], p[0], p[3], p[1], A1, ar, ai_);
      acc1 = __builtin_amdgcn_mfma_f32_16x16x4f32(ar, br, acc1, 0, 0, 0);
      acc1 = __builtin_amdgcn_mfma_f32_16x16x4f32(-ai_, bi, acc1, 0, 0, 0);
      cexp_rev(p[2], p[0], p[3], p[1], A2, ar, ai_);
      acc2 = __builtin_amdgcn_mfma_f32_16x16x4f32(ar, br, acc2, 0, 0, 0);
      acc2 = __builtin_amdgcn_mfma_f32_16x16x4f32(-ai_, bi, acc2, 0, 0, 0);
    }
#pragma unroll
    for (int r = 0; r < 4; ++r) {
      const int idx = (4 * kq + r) * 16 + i;
      part[(w * 3 + 0) * 256 + idx] = acc0[r];
      part[(w * 3 + 1) * 256 + idx] = acc1[r];
      part[(w * 3 + 2) * 256 + idx] = acc2[r];
    }
    __syncthreads();
#pragma unroll
    for (int s = 0; s < 3; ++s)
      ktab[s * 256 + tid] = 2.f * (part[(0 + s) * 256 + tid] + part[(3 + s) * 256 + tid] + part[(6 + s) * 256 + tid] +
                                   part[(9 + s) * 256 + tid]);
    __syncthreads();
    u16* rg = (u16*)(ws + WS_RG) + (size_t)d * RG_STRIDE;
    {
      const int q = tid;
      float v[8];
#pragma unroll
      for (int e = 0; e < 8; ++e) {
        const int l = 2047 - (8 * q + e);
        const int sh = l < 256 ? 0 : (l < 512 ? 1 : (l < 1024 ? 2 : 3));
        const int p = sh ? (((l + (1 << (sh - 1)) - 1 + ((l >> sh) & 1)) >> sh) << sh) : l;
        const int b = (p >> 3) & 15;
        int row;
        if (p < 256) row = 8 * (p >> 7) + (p & 7);
        else if (p < 512) row = 16 + 4 * ((p - 256) >> 7) + ((p & 7) >> 1);
        else if (p < 1024) row = 24 + 2 * ((p - 512) >> 7) + ((p & 7) >> 2);
        else row = 32 + ((p - 1024) >> 7);
        v[e] = ktab[row * 16 + b];
      }
      u32x4 o = {pk2(v[0], v[1]), pk2(v[2], v[3]), pk2(v[4], v[5]), pk2(v[6], v[7])};
      *(u32x4*)(rg + 8 * q) = o;
      if (tid < RG_STRIDE / 8 - 256) {
        u32x4 z = {0u, 0u, 0u, 0u};
        *(u32x4*)(rg + 2048 + 8 * tid) = z;
      }
    }
    return;
  }
  bid -= PREP_KGEN;
  if (bid < PREP_LN) {
    const int l0 = bid * 16;
    const int row = tid >> 4, t16 = tid & 15;
    const u16* src = xu + (size_t)(l0 + row) * D;
    u32x4 v[5];
#pragma unroll
    for (int i = 0; i < 5; ++i) v[i] = *(const u32x4*)(src + (t16 + 16 * i) * 8);
    float s = 0.f;
#pragma unroll
    for (int i = 0; i < 5; ++i) {
#pragma unroll
      for (int j = 0; j < 4; ++j) s += bflo(v[i][j]) + bfhi(v[i][j]);
      *(u32x4*)(smem + row * 1296 + (t16 + 16 * i) * 16) = v[i];
    }
    s += __shfl_xor(s, 1); s += __shfl_xor(s, 2); s += __shfl_xor(s, 4); s += __shfl_xor(s, 8);
    const float mean = s * (1.0f / D);
    float m2 = 0.f;
#pragma unroll
    for (int i = 0; i < 5; ++i)
#pragma unroll
      for (int j = 0; j < 4; ++j) {
        float a = bflo(v[i][j]) - mean, b = bfhi(v[i][j]) - mean;
        m2 += a * a + b * b;
      }
    m2 += __shfl_xor(m2, 1); m2 += __shfl_xor(m2, 2); m2 += __shfl_xor(m2, 4); m2 += __shfl_xor(m2, 8);
    if (t16 == 0) {
      float mb, sb;
      ln_params(s, m2, mb, sb);
      f32x2 o = {mb, sb};
      *(f32x2*)(ws + WS_ST0 + (size_t)(l0 + row) * 8) = o;
    }
    __syncthreads();
    u16* xt = (u16*)(ws + WS_XT0);
    const int g = lane >> 4, q = (lane & 15) >> 2, p = lane & 3, i16 = lane & 15;
#pragma unroll
    for (int it = 0; it < 5; ++it) {
      const int cb = (it * 4 + w) * 2 + (g >> 1);
      const char* base = smem + (8 * (g & 1) + q) * 1296 + cb * 32 + 8 * p;
      s16x4 lo = tr_read(base);
      s16x4 hi = tr_read(base + 4 * 1296);
      u32x2 a = __builtin_bit_cast(u32x2, lo), b = __builtin_bit_cast(u32x2, hi);
      u32x4 o = {a[0], a[1], b[0], b[1]};
      *(u32x4*)(xt + (size_t)(cb * 16 + i16) * LT + l0 + 8 * (g & 1)) = o;
    }
    return;
  }
  bid -= PREP_LN;
  if (bid < PREP_WQ + PREP_WO) {
    const bool isq = bid < PREP_WQ;
    const int t = isq ? bid : bid - PREP_WQ;
    const int ks = t / 5, nb = t % 5;
    const u16* src = (isq ? wq : wo) + (size_t)(32 * ks) * D + 128 * nb;
#pragma unroll
    for (int i = 0; i < 2; ++i) {
      const int c = tid + 256 * i, r = c >> 4, cc = c & 15;
      *(u32x4*)(smem + r * 272 + cc * 16) = *(const u32x4*)(src + (size_t)r * D + cc * 8);
    }
    __syncthreads();
    const int g = lane >> 4, q = (lane & 15) >> 2, p = lane & 3;
#pragma unroll
    for (int i = 0; i < 2; ++i) {
      const int ntl = w * 2 + i;
      const char* base = smem + (8 * g + q) * 272 + ntl * 32 + 8 * p;
      s16x4 lo = tr_read(base);
      s16x4 hi = tr_read(base + 4 * 272);
      u32x2 a = __builtin_bit_cast(u32x2, lo), b = __builtin_bit_cast(u32x2, hi);
      u32x4 o = {a[0], a[1], b[0], b[1]};
      *(u32x4*)(ws + (isq ? WS_WQ : WS_WO) + (size_t)((8 * nb + ntl) * 20 + ks) * 1024 + lane * 16) = o;
    }
    return;
  }
  bid -= PREP_WQ + PREP_WO;
  if (bid < PREP_WKV) {
    const int ks = bid, nt = 40 + w;
    const int n = (lane & 15), k0 = 32 * ks + 8 * (lane >> 4);
    const u16* src = (w < 2) ? wk : wv;
    const int col = 16 * (w & 1) + n;
    const bool valid = col < DH;
    unsigned e[8];
#pragma unroll
    for (int j = 0; j < 8; ++j) e[j] = valid ? (unsigned)src[(size_t)(k0 + j) * DH + col] : 0u;
    u32x4 o = {e[0] | (e[1] << 16), e[2] | (e[3] << 16), e[4] | (e[5] << 16), e[6] | (e[7] << 16)};
    *(u32x4*)(ws + WS_WQ + (size_t)(nt * 20 + ks) * 1024 + lane * 16) = o;
    return;
  }
  bid -= PREP_WKV;
  {
    if (bid == 0) {
      for (int c = tid; c < 3 * 64 * 32; c += 256) ((unsigned*)(ws + WS_CNT))[c] = 0u;
    }
    for (int c = bid * 256 + tid; c < 64 * 256; c += PREP_KV * 256) {
      const int fr = (c >> 6) & 3, ln = c & 63;
      unsigned v = (fr >= 2 && (ln & 31) == DH) ? 0x3f803f80u : 0u;
      u32x4 o = {v, v, v, v};
      *(u32x4*)(ws + WS_KV + (size_t)c * 16) = o;
    }
  }
}

constexpr int CS = 4160;
__global__ __launch_bounds__(256) void conv_kernel(const u16* __restrict__ xt, const f32x4* __restrict__ stats,
                                                   const u16* __restrict__ rg, const u16* __restrict__ dskip,
                                                   u16* __restrict__ xout) {
  __shared__ __attribute__((aligned(16))) char smem[8 * CS + 96 * 80];
  char* KC = smem;
  char* U = smem + 8 * CS;
  const int tid = threadIdx.x, lane = tid & 63, w = tid >> 6;
  const int d = blockIdx.x;
  const u32x4 xv = *(const u32x4*)(xt + (size_t)d * LT + 8 * tid);
  float mb[8], sb[8];
#pragma unroll
  for (int e = 0; e < 4; ++e) {
    const f32x4 s = stats[4 * tid + e];
    mb[2 * e] = s[0]; sb[2 * e] = s[1]; mb[2 * e + 1] = s[2]; sb[2 * e + 1] = s[3];
  }
  const u16* rgd = rg + (size_t)d * RG_STRIDE;
  const u32x4 r0 = *(const u32x4*)(rgd + 8 * tid);
  const u32x4 r1 = *(const u32x4*)(rgd + 8 * tid + 8);
  const float dsk = bf2f(dskip[d]);
  {
    unsigned wv[8] = {r0[0], r0[1], r0[2], r0[3], r1[0], r1[1], r1[2], r1[3]};
#pragma unroll
    for (int s = 0; s < 8; ++s) {
      u32x4 o;
#pragma unroll
      for (int k = 0; k < 4; ++k) {
        if (s & 1) o[k] = __builtin_amdgcn_alignbit(wv[(s >> 1) + k + 1], wv[(s >> 1) + k], 16);
        else o[k] = wv[(s >> 1) + k];
      }
      *(u32x4*)(KC + s * CS + tid * 16) = o;
    }
    if (tid < 32) {
      u32x4 z = {0u, 0u, 0u, 0u};
      *(u32x4*)(KC + (tid >> 2) * CS + (256 + (tid & 3)) * 16) = z;
    }
  }
  float xf[8], uf[8];
#pragma unroll
  for (int j = 0; j < 4; ++j) { xf[2 * j] = bflo(xv[j]); xf[2 * j + 1] = bfhi(xv[j]); }
#pragma unroll
  for (int e = 0; e < 8; ++e) uf[e] = rbf(rbf(xf[e] - mb[e]) * sb[e]);
  {
    u32x4 o = {pk2(uf[0], uf[1]), pk2(uf[2], uf[3]), pk2(uf[4], uf[5]), pk2(uf[6], uf[7])};
    *(u32x4*)(U + ((tid >> 2) + 32) * 80 + (tid & 3) * 16) = o;
    if (tid < 128) {
      u32x4 z = {0u, 0u, 0u, 0u};
      *(u32x4*)(U + (tid >> 2) * 80 + (tid & 3) * 16) = z;
    }
  }
  __syncthreads();
  const int b = lane & 31, h = lane >> 5;
  const char* kbase = KC + (7 - (b & 7)) * CS + 16 * (255 - (b >> 3) + h);
  const char* ubase = U + (b + 32) * 80 + 16 * h;
  f32x16 acc0 = {}, acc1 = {};
  int c0, c1, c2;
  if (w == 0) { c0 = 0; c1 = 12; c2 = 12; }
  else if (w == 1) { c0 = 12; c1 = 24; c2 = 24; }
  else if (w == 2) { c0 = 24; c1 = 32; c2 = 40; }
  else { c0 = 40; c1 = 40; c2 = 64; }
  for (int c = c0; c < c1; ++c) {
#pragma unroll
    for (int ks = 0; ks < 2; ++ks) {
      const bf16x8 kf = *(const bf16x8*)(kbase - 64 * c + 32 * ks);
      const bf16x8 u0 = *(const bf16x8*)(ubase - 80 * c + 32 * ks);
      const bf16x8 u1 = *(const bf16x8*)(ubase + 80 * (32 - c) + 32 * ks);
      acc0 = __builtin_amdgcn_mfma_f32_32x32x16_bf16(u0, kf, acc0, 0, 0, 0);
      acc1 = __builtin_amdgcn_mfma_f32_32x32x16_bf16(u1, kf, acc1, 0, 0, 0);
    }
  }
  for (int c = c1; c < c2; ++c) {
#pragma unroll
    for (int ks = 0; ks < 2; ++ks) {
      const bf16x8 kf = *(const bf16x8*)(kbase - 64 * c + 32 * ks);
      const bf16x8 u1 = *(const bf16x8*)(ubase + 80 * (32 - c) + 32 * ks);
      acc1 = __builtin_amdgcn_mfma_f32_32x32x16_bf16(u1, kf, acc1, 0, 0, 0);
    }
  }
  __syncthreads();
  float* part = (float*)smem;
#pragma unroll
  for (int r = 0; r < 16; ++r) {
    const int a = crow(r, h);
    part[w * 2048 + 32 * a + b] = acc0[r];
    part[w * 2048 + 1024 + 32 * a + b] = acc1[r];
  }
  __syncthreads();
  float y[8];
#pragma unroll
  for (int e = 0; e < 8; ++e) y[e] = 0.f;
#pragma unroll
  for (int ww = 0; ww < 4; ++ww) {
    const f32x4 p0 = *(const f32x4*)(part + ww * 2048 + 8 * tid);
    const f32x4 p1 = *(const f32x4*)(part + ww * 2048 + 8 * tid + 4);
#pragma unroll
    for (int e = 0; e < 4; ++e) { y[e] += p0[e]; y[4 + e] += p1[e]; }
  }
  float o[8];
#pragma unroll
  for (int e = 0; e < 8; ++e) o[e] = xf[e] + rbf(rbf(y[e]) + rbf(uf[e] * dsk));
  u32x4 ov = {pk2(o[0], o[1]), pk2(o[2], o[3]), pk2(o[4], o[5]), pk2(o[6], o[7])};
  *(u32x4*)(xout + (size_t)d * LT + 8 * tid) = ov;
}

__global__ __launch_bounds__(512, 2) void qkv_kernel(const u16* __restrict__ xt, const char* __restrict__ wf,
                                                     u16* __restrict__ Q, char* __restrict__ kvf) {
  __shared__ __attribute__((aligned(16))) char smem[640 * 128 + 512 + 6 * 5 * 1024];
  f32x2* lnp = (f32x2*)(smem + 640 * 128);
  const int tid = threadIdx.x, lane = tid & 63, w = tid >> 6;
  const int idx = blockIdx.x >> 3;
  const int rt = (idx >> 3) * 8 + (blockIdx.x & 7), ns = idx & 7;
  const int l0 = 64 * rt;
  {
    const int dr = lane >> 3, c = lane & 7;
    const int sw = ((dr >> 1) & 1) | ((w & 1) << 1);
#pragma unroll
    for (int i = 0; i < 10; ++i) {
      const int piece = i * 8 + w;
      const int d = 8 * piece + dr;
      const u16* src = xt + (size_t)d * LT + l0 + 8 * (c ^ (sw << 1));
      __builtin_amdgcn_global_load_lds((const __attribute__((address_space(1))) void*)src,
                                       (__attribute__((address_space(3))) void*)(smem + piece * 1024), 16, 0, 0);
    }
  }
  const int P = w % 3, Hh = w / 3;
  const int ntp = ns * 6 + 2 * P;
  const bool active = (w < 6) && (ntp < 44);
  const char* wbase = wf + (size_t)((active ? ntp : 0) * 20 + 10 * Hh) * 1024 + lane * 16;
  bf16x8 w0[10], w1[10];
  if (active) {
#pragma unroll
    for (int ks = 0; ks < 10; ++ks) w0[ks] = *(const bf16x8*)(wbase + ks * 1024);
#pragma unroll
    for (int ks = 0; ks < 10; ++ks) w1[ks] = *(const bf16x8*)(wbase + 20 * 1024 + ks * 1024);
  }
  __syncthreads();
  const int g = lane >> 4, q = (lane & 15) >> 2, p = lane & 3;
  const int sw = ((q >> 1) & 1) | ((g & 1) << 1);
  const char* ub = smem + (8 * g + q) * 128 + 8 * p;
  const u32x4 onesu = {0x3f803f80u, 0x3f803f80u, 0x3f803f80u, 0x3f803f80u};
  const bf16x8 ones = __builtin_bit_cast(bf16x8, onesu);
  f32x4 acc[4], accc = {0.f, 0.f, 0.f, 0.f};
  f32x4* xch = (f32x4*)(smem + 640 * 128 + 512);
#pragma unroll
  for (int a = 0; a < 4; ++a) acc[a] = f32x4{};
  if (active) {
    f32x4 bcc[4], bccc = {0.f, 0.f, 0.f, 0.f};
#pragma unroll
    for (int a = 0; a < 4; ++a) bcc[a] = f32x4{};
    const char* ubh = ub + Hh * 10 * 4096;
#pragma unroll
    for (int ks = 0; ks < 10; ++ks) {
#pragma unroll
      for (int lt = 0; lt < 4; ++lt) {
        const char* pp = ubh + ks * 4096 + ((lt ^ sw) << 5);
        s16x4 a0 = tr_read(pp), a1 = tr_read(pp + 512);
        bf16x8 u0 = __builtin_bit_cast(bf16x8, __builtin_shufflevector(a0, a1, 0, 1, 2, 3, 4, 5, 6, 7));
        acc[lt] = __builtin_amdgcn_mfma_f32_16x16x32_bf16(w0[ks], u0, acc[lt], 0, 0, 0);
        bcc[lt] = __builtin_amdgcn_mfma_f32_16x16x32_bf16(w1[ks], u0, bcc[lt], 0, 0, 0);
      }
      accc = __builtin_amdgcn_mfma_f32_16x16x32_bf16(w0[ks], ones, accc, 0, 0, 0);
      bccc = __builtin_amdgcn_mfma_f32_16x16x32_bf16(w1[ks], ones, bccc, 0, 0, 0);
    }
    if (Hh) {
#pragma unroll
      for (int a = 0; a < 4; ++a) { xch[(w * 5 + a) * 64 + lane] = acc[a]; acc[a] = bcc[a]; }
      xch[(w * 5 + 4) * 64 + lane] = accc; accc = bccc;
    } else {
#pragma unroll
      for (int a = 0; a < 4; ++a) xch[(w * 5 + a) * 64 + lane] = bcc[a];
      xch[(w * 5 + 4) * 64 + lane] = bccc;
    }
  } else if (w >= 6) {
#pragma unroll
    for (int ks = 0; ks < 20; ++ks) {
#pragma unroll
      for (int j = 0; j < 2; ++j) {
        const int lt = 2 * (w - 6) + j;
        const char* pp = ub + ks * 4096 + ((lt ^ sw) << 5);
        s16x4 a0 = tr_read(pp), a1 = tr_read(pp + 512);
        bf16x8 u0 = __builtin_bit_cast(bf16x8, __builtin_shufflevector(a0, a1, 0, 1, 2, 3, 4, 5, 6, 7));
        acc[j] = __builtin_amdgcn_mfma_f32_16x16x32_bf16(ones, u0, acc[j], 0, 0, 0);
        acc[2 + j] = __builtin_amdgcn_mfma_f32_16x16x32_bf16(u0, u0, acc[2 + j], 0, 0, 0);
      }
    }
    const int li = lane & 15;
    if (g == (li >> 2)) {
#pragma unroll
      for (int j = 0; j < 2; ++j) {
        const float sx = acc[j][0];
        const f32x4 gm = acc[2 + j];
        const int r = li & 3;
        const float sxx = r == 0 ? gm[0] : (r == 1 ? gm[1] : (r == 2 ? gm[2] : gm[3]));
        float mb, sb;
        ln_params(sx, fmaxf(sxx - sx * sx * (1.0f / D), 0.f), mb, sb);
        f32x2 o = {mb, sb};
        lnp[16 * (2 * (w - 6) + j) + li] = o;
      }
    }
  }
  __syncthreads();
  if (!active) return;
  const int nt = ntp + Hh;
  {
    const int pw = Hh ? w - 3 : w + 3;
#pragma unroll
    for (int a = 0; a < 4; ++a) acc[a] += xch[(pw * 5 + a) * 64 + lane];
    accc += xch[(pw * 5 + 4) * 64 + lane];
  }
#pragma unroll
  for (int lt = 0; lt < 4; ++lt) {
    const f32x2 pr = lnp[16 * lt + (lane & 15)];
#pragma unroll
    for (int e = 0; e < 4; ++e) acc[lt][e] = (acc[lt][e] - pr[0] * accc[e]) * pr[1];
  }
  const float qscale = 0.22360679774997896f * 1.4426950408889634f;
#pragma unroll
  for (int lt = 0; lt < 4; ++lt) {
    const int l = l0 + 16 * lt + (lane & 15);
    const f32x4 r = acc[lt];
    if (nt < 40) {
      u32x2 o = {pk2(r[0] * qscale, r[1] * qscale), pk2(r[2] * qscale, r[3] * qscale)};
      *(u32x2*)(Q + (size_t)l * D + 16 * nt + 4 * g) = o;
    } else if (nt < 42) {
      const int r32 = l & 31;
      u32x2 o = {pk2(r[0], r[1]), pk2(r[2], r[3])};
      *(u32x2*)(kvf + (size_t)(l >> 5) * 4096 + (nt - 40) * 1024 + (r32 + 32 * (g >> 1)) * 16 + 8 * (g & 1)) = o;
    } else {
      const int r32 = l & 31;
      const int s_ = r32 >> 4, jh = (r32 >> 3) & 1, hh = (r32 >> 2) & 1, jl = r32 & 3;
      const int j = 4 * jh + jl;
#pragma unroll
      for (int e = 0; e < 4; ++e) {
        const int dv = 16 * (nt - 42) + 4 * g + e;
        if (dv < DH) {
          u16 bv = (u16)(pk2(r[e], 0.f) & 0xffffu);
          *(u16*)(kvf + (size_t)(l >> 5) * 4096 + (2 + s_) * 1024 + (dv + 32 * hh) * 16 + 2 * j) = bv;
        }
      }
    }
  }
}

constexpr int ACH = 8;
constexpr int WLK = 9;
DI void attn_pack(const f32x16& s, float delta, u32x4& pa, u32x4& pb) {
#pragma unroll
  for (int r = 0; r < 4; ++r) {
    pa[r] = pk2(__builtin_amdgcn_exp2f(s[2 * r] - delta), __builtin_amdgcn_exp2f(s[2 * r + 1] - delta));
    pb[r] = pk2(__builtin_amdgcn_exp2f(s[8 + 2 * r] - delta), __builtin_amdgcn_exp2f(s[8 + 2 * r + 1] - delta));
  }
}
DI void attn_safe_tile(const char* tb, const bf16x8& qf0, const bf16x8& qf1, f32x16& o, f32x16& negm, bool first) {
  const bf16x8 k0 = *(const bf16x8*)(tb);
  const bf16x8 k1 = *(const bf16x8*)(tb + 1024);
  const bf16x8 v0 = *(const bf16x8*)(tb + 2048);
  const bf16x8 v1 = *(const bf16x8*)(tb + 3072);
  f32x16 s = __builtin_amdgcn_mfma_f32_32x32x16_bf16(k0, qf0, negm, 0, 0, 0);
  s = __builtin_amdgcn_mfma_f32_32x32x16_bf16(k1, qf1, s, 0, 0, 0);
  float tm = s[0];
#pragma unroll
  for (int r = 1; r < 16; ++r) tm = fmaxf(tm, s[r]);
  tm = fmaxf(tm, __shfl_xor(tm, 32));
  const float delta = first ? tm : fmaxf(tm, 0.f);
  const float sc = first ? 0.f : __builtin_amdgcn_exp2f(-delta);
#pragma unroll
  for (int r = 0; r < 16; ++r) { o[r] *= sc; negm[r] -= delta; }
  u32x4 pa, pb;
  attn_pack(s, delta, pa, pb);
  o = __builtin_amdgcn_mfma_f32_32x32x16_bf16(v0, __builtin_bit_cast(bf16x8, pa), o, 0, 0, 0);
  o = __builtin_amdgcn_mfma_f32_32x32x16_bf16(v1, __builtin_bit_cast(bf16x8, pb), o, 0, 0, 0);
}
#define SBAR() __builtin_amdgcn_sched_barrier(0)
#define EXP4(P, I, S, B)                                                                   \
  P[I] = pk2(__builtin_amdgcn_exp2f(S[B]), __builtin_amdgcn_exp2f(S[B + 1]));              \
  P[I + 1] = pk2(__builtin_amdgcn_exp2f(S[B + 2]), __builtin_amdgcn_exp2f(S[B + 3]));
template <int T0>
DI void attn_fast_chunk(const char* base, const bf16x8& qf0, const bf16x8& qf1, f32x16& o, const f32x16& negm) {
  f32x16 s_cur, s_nxt;
  bf16x8 k0, k1, v0, v1, v1p, kn0, kn1, vn0, vn1;
  u32x4 pa, pb, pbp;
  {
    k0 = *(const bf16x8*)(base + T0 * 4096);
    k1 = *(const bf16x8*)(base + T0 * 4096 + 1024);
    v0 = *(const bf16x8*)(base + T0 * 4096 + 2048);
    v1 = *(const bf16x8*)(base + T0 * 4096 + 3072);
    s_cur = __builtin_amdgcn_mfma_f32_32x32x16_bf16(k0, qf0, negm, 0, 0, 0);
    s_cur = __builtin_amdgcn_mfma_f32_32x32x16_bf16(k1, qf1, s_cur, 0, 0, 0);
    k0 = *(const bf16x8*)(base + (T0 + 1) * 4096);
    k1 = *(const bf16x8*)(base + (T0 + 1) * 4096 + 1024);
  }
#pragma unroll
  for (int t = T0; t < ACH; ++t) {
    SBAR();
    if (t + 1 < ACH) s_nxt = __builtin_amdgcn_mfma_f32_32x32x16_bf16(k0, qf0, negm, 0, 0, 0);
    if (t + 2 < ACH) {
      kn0 = *(const bf16x8*)(base + (t + 2) * 4096);
      kn1 = *(const bf16x8*)(base + (t + 2) * 4096 + 1024);
    }
    if (t + 1 < ACH) {
      vn0 = *(const bf16x8*)(base + (t + 1) * 4096 + 2048);
      vn1 = *(const bf16x8*)(base + (t + 1) * 4096 + 3072);
    }
    SBAR();
    EXP4(pa, 0, s_cur, 0);
    SBAR();
    if (t > T0) o = __builtin_amdgcn_mfma_f32_32x32x16_bf16(v1p, __builtin_bit_cast(bf16x8, pbp), o, 0, 0, 0);
    SBAR();
    EXP4(pa, 2, s_cur, 4);
    SBAR();
    if (t + 1 < ACH) s_nxt = __builtin_amdgcn_mfma_f32_32x32x16_bf16(k1, qf1, s_nxt, 0, 0, 0);
    SBAR();
    EXP4(pb, 0, s_cur, 8);
    SBAR();
    o = __builtin_amdgcn_mfma_f32_32x32x16_bf16(v0, __builtin_bit_cast(bf16x8, pa), o, 0, 0, 0);
    SBAR();
    EXP4(pb, 2, s_cur, 12);
    pbp = pb; v1p = v1;
    s_cur = s_nxt; k0 = kn0; k1 = kn1; v0 = vn0; v1 = vn1;
  }
  SBAR();
  o = __builtin_amdgcn_mfma_f32_32x32x16_bf16(v1p, __builtin_bit_cast(bf16x8, pbp), o, 0, 0, 0);
  SBAR();
}

typedef __attribute__((address_space(1))) unsigned gu32;
typedef __attribute__((address_space(1))) unsigned long long gu64;
#define RLX_AGENT __ATOMIC_RELAXED, __HIP_MEMORY_SCOPE_AGENT

template <bool FINAL>
__global__ __launch_bounds__(512, 2) void attn_kernel(const u16* __restrict__ Q, const char* __restrict__ kvf,
                                                      u16* O, const char* __restrict__ wf,
                                                      const u16* __restrict__ xres, u16* __restrict__ xout,
                                                      f32x2* pstats, f32x2* __restrict__ fstats, unsigned* cnt) {
  __shared__ __attribute__((aligned(16))) char smem[2 * ACH * 4096 + 10 * WLK * 1024];
  const int tid = threadIdx.x, lane = tid & 63, w = tid >> 6;
  const int hq = lane & 31, h = lane >> 5;
  const int idx = blockIdx.x >> 3;
  const int rt = (idx >> 2) * 8 + (blockIdx.x & 7), ns = idx & 3;
  const int row = 32 * rt + 8 * ns + w;
  auto stage = [&](int chunk, int buf) {
#pragma unroll
    for (int i = 0; i < ACH / 2; ++i) {
      const char* src = kvf + (size_t)chunk * ACH * 4096 + w * (ACH * 512) + i * 1024 + lane * 16;
      __builtin_amdgcn_global_load_lds((const __attribute__((address_space(1))) void*)src,
                                       (__attribute__((address_space(3))) void*)(smem + buf * ACH * 4096 + w * (ACH * 512) + i * 1024),
                                       16, 0, 0);
    }
  };
  stage(0, 0);
  bf16x8 qf0, qf1;
  {
    const u16* qp = Q + (size_t)row * D + hq * DH;
    u32x2 a = *(const u32x2*)(qp + 8 * h), b = *(const u32x2*)(qp + 8 * h + 4);
    u32x4 t = {a[0], a[1], b[0], b[1]};
    qf0 = __builtin_bit_cast(bf16x8, t);
    u32x2 c = *(const u32x2*)(qp + 16);
    u32x4 t1 = {h ? 0u : c[0], h ? 0u : c[1], 0u, 0u};
    qf1 = __builtin_bit_cast(bf16x8, t1);
  }
  f32x16 o = {};
  f32x16 negm = {};
  const int l0 = 32 * rt;
  const int g = lane >> 4, i16 = lane & 15;
  const int nt0 = ns * 10 + 2 * (w < 5 ? w : 0);
  bf16x8 w0[20 - WLK], w1[20 - WLK];
  u32x2 xr[2][2];
  u16 xs[2][2][4];
  __syncthreads();
  auto guard = [&](const char* base, const f32x16& ock, int t0) {
    const float lsum = h ? o[8] : 0.f;
    if (__builtin_expect(__any(!(lsum < 65536.f)), 0)) {
      if (__any(!(lsum < 1.8446744e19f))) {
        o = ock;
        for (int t = t0; t < ACH; ++t) attn_safe_tile(base + t * 4096, qf0, qf1, o, negm, false);
      }
      float ls = __shfl(o[8], hq + 32);
      int e = (int)((__float_as_uint(ls) >> 23) & 0xff) - 127;
      e = (ls > 1.f && e < 128) ? e : 0;
      const float sc = __uint_as_float((unsigned)(127 - e) << 23);
      const float fe = (float)e;
#pragma unroll
      for (int r = 0; r < 16; ++r) { o[r] *= sc; negm[r] -= fe; }
    }
  };
  const char* wbase = wf + (size_t)(nt0 * 20 + WLK) * 1024 + lane * 16;
  for (int ch = 0; ch < 64 / ACH - 2; ++ch) {
    stage(ch + 1, (ch + 1) & 1);
    if (ch < 5) {
      for (int f = ch * 2 * WLK + w; f < (ch + 1) * 2 * WLK; f += 8) {
        const int ntl = f / WLK, ks = f % WLK;
        const char* src = wf + (size_t)((ns * 10 + ntl) * 20 + ks) * 1024 + lane * 16;
        __builtin_amdgcn_global_load_lds((const __attribute__((address_space(1))) void*)src,
                                         (__attribute__((address_space(3))) void*)(smem + 2 * ACH * 4096 + f * 1024), 16, 0, 0);
      }
    }
    const char* base = smem + (ch & 1) * ACH * 4096 + lane * 16;
    f32x16 ock;
    if (ch == 0) {
      attn_safe_tile(base, qf0, qf1, o, negm, true);
      ock = o;
      attn_fast_chunk<1>(base, qf0, qf1, o, negm);
    } else {
      ock = o;
      attn_fast_chunk<0>(base, qf0, qf1, o, negm);
    }
    guard(base, ock, ch == 0 ? 1 : 0);
    __syncthreads();
  }
  {
    stage(64 / ACH - 1, (64 / ACH - 1) & 1);
    if (w < 5) {
#pragma unroll
      for (int ks = 0; ks < 20 - WLK; ++ks) w0[ks] = *(const bf16x8*)(wbase + ks * 1024);
    }
    const char* base = smem + ((64 / ACH - 2) & 1) * ACH * 4096 + lane * 16;
    const f32x16 ock = o;
    attn_fast_chunk<0>(base, qf0, qf1, o, negm);
    guard(base, ock, 0);
    __syncthreads();
  }
  {
    if (w < 5) {
#pragma unroll
      for (int ks = 0; ks < 20 - WLK; ++ks) w1[ks] = *(const bf16x8*)(wbase + 20 * 1024 + ks * 1024);
#pragma unroll
      for (int a = 0; a < 2; ++a)
#pragma unroll
        for (int lt = 0; lt < 2; ++lt) {
          if (FINAL) {
#pragma unroll
            for (int e = 0; e < 4; ++e) xs[a][lt][e] = xres[(size_t)(16 * (nt0 + a) + 4 * g + e) * LT + l0 + 16 * lt + i16];
          } else {
            xr[a][lt] = *(const u32x2*)(xres + (size_t)(16 * (nt0 + a) + i16) * LT + l0 + 16 * lt + 4 * g);
          }
        }
    }
    const char* base = smem + ((64 / ACH - 1) & 1) * ACH * 4096 + lane * 16;
    const f32x16 ock = o;
    attn_fast_chunk<0>(base, qf0, qf1, o, negm);
    guard(base, ock, 0);
    __syncthreads();
  }
  {
    float lsum = o[8];
    lsum = __shfl(lsum, hq + 32);
    const float inv = 1.0f / lsum;
#pragma unroll
    for (int g = 0; g < 3; ++g) {
      const int dv = 8 * g + 4 * h;
      if (dv < DH) {
        const int k = hq * DH + dv;
        u32x2 ov = {pk2(o[4 * g] * inv, o[4 * g + 1] * inv), pk2(o[4 * g + 2] * inv, o[4 * g + 3] * inv)};
        *(u32x2*)(smem + ((k >> 3) * 8 + w) * 16 + (k & 7) * 2) = ov;
      }
    }
    __syncthreads();
    const __amdgpu_buffer_rsrc_t rs = __builtin_amdgcn_make_buffer_rsrc((void*)O, 0, L * D * 2, 0x00020000);
    const unsigned obase = (unsigned)((row >> 4) * 20480 + (8 * (ns & 1)) * 16);
#pragma unroll
    for (int i = 0; i < 2; ++i) {
      const int c = tid + 512 * i;
      if (c < 640) {
        const u32x4 t = *(const u32x4*)(smem + c * 16);
        __builtin_amdgcn_raw_buffer_store_b128(t, rs, obase + (unsigned)((c >> 3) * 256 + (c & 7) * 16), 0, 16);
      }
    }
  }
  asm volatile("" ::: "memory");
  asm volatile("s_waitcnt vmcnt(0)" ::: "memory");
  __builtin_amdgcn_s_barrier();
  gu32* c0 = (gu32*)cnt + rt * 32;
  if (tid == 448) {
    __hip_atomic_fetch_add(c0, 1u, RLX_AGENT);
    for (unsigned spins = 0; spins < (1u << 22); ++spins) {
      if (__hip_atomic_load(c0, RLX_AGENT) >= 4u) break;
      __builtin_amdgcn_s_sleep(1);
    }
  }
  asm volatile("" ::: "memory");
  __builtin_amdgcn_s_barrier();
  asm volatile("" ::: "memory");
  {
    const __amdgpu_buffer_rsrc_t rs = __builtin_amdgcn_make_buffer_rsrc((void*)O, 0, L * D * 2, 0x00020000);
    u32x4 t[5];
#pragma unroll
    for (int i = 0; i < 5; ++i)
      t[i] = __builtin_amdgcn_raw_buffer_load_b128(rs, (unsigned)(rt * 40960 + (w * 5 + i) * 1024 + lane * 16), 0, 16);
#pragma unroll
    for (int i = 0; i < 5; ++i) *(u32x4*)(smem + (w * 5 + i) * 1024 + lane * 16) = t[i];
  }
  __syncthreads();
  f32x4 acc[2][2];
  acc[0][0] = f32x4{}; acc[0][1] = f32x4{}; acc[1][0] = f32x4{}; acc[1][1] = f32x4{};
  float* st = (float*)(smem + 40960);
  if (w < 5) {
    const char* obase = smem + lane * 16;
    const char* wl = smem + 2 * ACH * 4096 + (2 * w) * WLK * 1024 + lane * 16;
#pragma unroll
    for (int ks = 0; ks < 20; ++ks) {
      const bf16x8 o0 = *(const bf16x8*)(obase + ks * 1024);
      const bf16x8 o1 = *(const bf16x8*)(obase + 20 * 1024 + ks * 1024);
      const bf16x8 wa = ks < WLK ? *(const bf16x8*)(wl + ks * 1024) : w0[ks < WLK ? 0 : ks - WLK];
      const bf16x8 wb = ks < WLK ? *(const bf16x8*)(wl + (WLK + ks) * 1024) : w1[ks < WLK ? 0 : ks - WLK];
      if (FINAL) {
        acc[0][0] = __builtin_amdgcn_mfma_f32_16x16x32_bf16(wa, o0, acc[0][0], 0, 0, 0);
        acc[0][1] = __builtin_amdgcn_mfma_f32_16x16x32_bf16(wa, o1, acc[0][1], 0, 0, 0);
        acc[1][0] = __builtin_amdgcn_mfma_f32_16x16x32_bf16(wb, o0, acc[1][0], 0, 0, 0);
        acc[1][1] = __builtin_amdgcn_mfma_f32_16x16x32_bf16(wb, o1, acc[1][1], 0, 0, 0);
      } else {
        acc[0][0] = __builtin_amdgcn_mfma_f32_16x16x32_bf16(o0, wa, acc[0][0], 0, 0, 0);
        acc[0][1] = __builtin_amdgcn_mfma_f32_16x16x32_bf16(o1, wa, acc[0][1], 0, 0, 0);
        acc[1][0] = __builtin_amdgcn_mfma_f32_16x16x32_bf16(o0, wb, acc[1][0], 0, 0, 0);
        acc[1][1] = __builtin_amdgcn_mfma_f32_16x16x32_bf16(o1, wb, acc[1][1], 0, 0, 0);
      }
    }
    if (FINAL) {
#pragma unroll
      for (int a = 0; a < 2; ++a)
#pragma unroll
        for (int lt = 0; lt < 2; ++lt) {
          const int l = l0 + 16 * lt + i16;
          const int n = 16 * (nt0 + a) + 4 * g;
          float r[4];
#pragma unroll
          for (int e = 0; e < 4; ++e) r[e] = bf2f(xs[a][lt][e]) + rbf(acc[a][lt][e]);
          u32x2 ov = {pk2(r[0], r[1]), pk2(r[2], r[3])};
          *(u32x2*)(xout + (size_t)l * D + n) = ov;
        }
    } else {
#pragma unroll
      for (int a = 0; a < 2; ++a)
#pragma unroll
        for (int lt = 0; lt < 2; ++lt) {
          const int n = 16 * (nt0 + a) + i16;
          const int l = l0 + 16 * lt + 4 * g;
          float r[4];
          r[0] = rbf(bflo(xr[a][lt][0]) + rbf(acc[a][lt][0]));
          r[1] = rbf(bfhi(xr[a][lt][0]) + rbf(acc[a][lt][1]));
          r[2] = rbf(bflo(xr[a][lt][1]) + rbf(acc[a][lt][2]));
          r[3] = rbf(bfhi(xr[a][lt][1]) + rbf(acc[a][lt][3]));
          u32x2 ov = {pk2(r[0], r[1]), pk2(r[2], r[3])};
          *(u32x2*)(xout + (size_t)n * LT + l) = ov;
          const int nl = 16 * (2 * w + a) + i16;
#pragma unroll
          for (int e = 0; e < 4; ++e) st[(16 * lt + 4 * g + e) * 164 + nl] = r[e];
        }
    }
  }
  if (!FINAL) {
    __syncthreads();
    unsigned* flag = (unsigned*)(smem + 40960 + 32 * 164 * 4);
    if (tid < 256) {
      const int l = tid >> 3, p8 = tid & 7;
      float v[20];
      float s = 0.f;
#pragma unroll
      for (int e = 0; e < 20; ++e) { v[e] = st[l * 164 + p8 + 8 * e]; s += v[e]; }
      s += __shfl_xor(s, 1); s += __shfl_xor(s, 2); s += __shfl_xor(s, 4);
      const float mean = s * (1.0f / 160.f);
      float m2 = 0.f;
#pragma unroll
      for (int e = 0; e < 20; ++e) { float a = v[e] - mean; m2 += a * a; }
      m2 += __shfl_xor(m2, 1); m2 += __shfl_xor(m2, 2); m2 += __shfl_xor(m2, 4);
      if (p8 == 0) {
        const unsigned long long pv = (unsigned long long)__float_as_uint(s) | ((unsigned long long)__float_as_uint(m2) << 32);
        __hip_atomic_store((gu64*)(pstats + (size_t)(l0 + l) * 4 + ns), pv, RLX_AGENT);
      }
      asm volatile("s_waitcnt vmcnt(0)" ::: "memory");
    }
    __syncthreads();
    if (tid == 0) *flag = __hip_atomic_fetch_add((gu32*)cnt + (64 + rt) * 32, 1u, RLX_AGENT);
    __syncthreads();
    if (*flag == 3u && tid < 32) {
      float ps[4], pm[4];
      float sum = 0.f;
#pragma unroll
      for (int p = 0; p < 4; ++p) {
        const unsigned long long pv = __hip_atomic_load((gu64*)(pstats + (size_t)(l0 + tid) * 4 + p), RLX_AGENT);
        ps[p] = __uint_as_float((unsigned)pv); pm[p] = __uint_as_float((unsigned)(pv >> 32));
        sum += ps[p];
      }
      const float mean = sum * (1.0f / D);
      float m2 = 0.f;
#pragma unroll
      for (int p = 0; p < 4; ++p) {
        const float dm = ps[p] * (4.0f / D) - mean;
        m2 += pm[p] + dm * dm * (D / 4.0f);
      }
      float mb, sb;
      ln_params(sum, m2, mb, sb);
      f32x2 ov = {mb, sb};
      fstats[l0 + tid] = ov;
    }
  }
}

extern "C" void kernel_launch(void* const* d_in, const int* in_sizes, int n_in, void* d_out, int out_size,
                              void* d_ws, size_t ws_size, hipStream_t stream) {
  const u16* xu = (const u16*)d_in[0];
  const u16* lar = (const u16*)d_in[1];
  const u16* aim = (const u16*)d_in[2];
  const u16* cre = (const u16*)d_in[3];
  const u16* cim = (const u16*)d_in[4];
  const u16* ldt = (const u16*)d_in[5];
  const u16* dsk = (const u16*)d_in[6];
  const u16* wq = (const u16*)d_in[8];
  const u16* wk = (const u16*)d_in[9];
  const u16* wv = (const u16*)d_in[10];
  const u16* wo = (const u16*)d_in[11];
  char* ws = (char*)d_ws;
  u16* out = (u16*)d_out;
  const u16* rg = (const u16*)(ws + WS_RG);
  u16* xt0 = (u16*)(ws + WS_XT0);
  u16* xt1 = (u16*)(ws + WS_XT1);
  u16* xt2 = (u16*)(ws + WS_XT2);
  u16* xt3 = (u16*)(ws + WS_XT3);
  f32x2* st0 = (f32x2*)(ws + WS_ST0);
  f32x2* st2 = (f32x2*)(ws + WS_ST2);
  u16* Q = (u16*)(ws + WS_Q);
  u16* O = (u16*)(ws + WS_O);
  char* kvf = ws + WS_KV;

  f32x2* sf2 = (f32x2*)(ws + WS_SF2);
  unsigned* cnt = (unsigned*)(ws + WS_CNT);
  prep_kernel<<<PREP_BLOCKS, 256, 0, stream>>>(xu, lar, aim, cre, cim, ldt, wq, wk, wv, wo, ws);
  conv_kernel<<<D, 256, 0, stream>>>(xt0, (const f32x4*)st0, rg, dsk, xt1);
  qkv_kernel<<<256, 512, 0, stream>>>(xt1, ws + WS_WQ, Q, kvf);
  attn_kernel<false><<<256, 512, 0, stream>>>(Q, kvf, O, ws + WS_WO, xt1, xt2, st2, sf2, cnt);
  conv_kernel<<<D, 256, 0, stream>>>(xt2, (const f32x4*)sf2, rg, dsk, xt3);
  qkv_kernel<<<256, 512, 0, stream>>>(xt3, ws + WS_WQ, Q, kvf);
  attn_kernel<true><<<256, 512, 0, stream>>>(Q, kvf, O, ws + WS_WO, xt3, out, st2, sf2, cnt + 128 * 32);
}
```

```cpp
#include <hip/hip_runtime.h>
#include <hip/hip_bf16.h>
#include <stdint.h>

typedef __attribute__((ext_vector_type(8))) __bf16 bf16x8;
typedef __attribute__((ext_vector_type(2))) __bf16 bf16x2;
typedef __attribute__((ext_vector_type(4))) short s16x4;
typedef __attribute__((ext_vector_type(16))) float f32x16;
typedef __attribute__((ext_vector_type(4))) float f32x4;
typedef __attribute__((ext_vector_type(2))) float f32x2;
typedef __attribute__((ext_vector_type(4))) unsigned u32x4;
typedef __attribute__((ext_vector_type(2))) unsigned u32x2;
typedef unsigned short u16;
#define DI __device__ __forceinline__

constexpr int L = 2048, D = 640, NS = 128, H = 32, DH = 20;
constexpr int RG_STRIDE = 2112;

constexpr int LT = 2112;
constexpr size_t SZ_XT = (size_t)D * LT * 2;
constexpr size_t WS_RG = 0;
constexpr size_t WS_XT0 = WS_RG + (size_t)D * RG_STRIDE * 2;
constexpr size_t WS_XT1 = WS_XT0 + SZ_XT;
constexpr size_t WS_XT2 = WS_XT1 + SZ_XT;
constexpr size_t WS_XT3 = WS_XT2 + SZ_XT;
constexpr size_t WS_ST0 = WS_XT3 + SZ_XT;
constexpr size_t WS_ST2 = WS_ST0 + (size_t)L * 8;
constexpr size_t WS_SF2 = WS_ST2 + (size_t)L * 4 * 8;
constexpr size_t WS_CNT = WS_SF2 + (size_t)L * 8;
constexpr size_t WS_CS = WS_CNT + (size_t)3 * 64 * 128;
constexpr size_t WS_WQ = WS_CS + 4096;
constexpr size_t WS_WO = WS_WQ + (size_t)44 * 20 * 1024;
constexpr size_t WS_Q = WS_WO + (size_t)40 * 20 * 1024;
constexpr size_t WS_KV = WS_Q + SZ_XT;
constexpr size_t WS_O = WS_KV + (size_t)64 * 4096;
constexpr size_t WS_END = WS_O + SZ_XT;

#define STAMP(kid, i) do {} while (0)
DI float bf2f(u16 v) { return __uint_as_float(((unsigned)v) << 16); }
DI float bflo(unsigned w) { return __uint_as_float(w << 16); }
DI float bfhi(unsigned w) { return __uint_as_float(w & 0xffff0000u); }
DI unsigned pk2(float a, float b) {
  f32x2 f = {a, b};
  bf16x2 h = __builtin_convertvector(f, bf16x2);
  return __builtin_bit_cast(unsigned, h);
}
DI float rbf(float a) {
  return __uint_as_float(pk2(a, 0.f) << 16);
}
DI int crow(int reg, int h) { return (reg & 3) + 8 * (reg >> 2) + 4 * h; }
typedef __attribute__((address_space(3))) s16x4 lds_s16x4;
DI s16x4 tr_read(const char* p) { return __builtin_amdgcn_ds_read_tr16_b64_v4i16((lds_s16x4*)p); }

DI void cexpf_(float re, float im, float p, float& er, float& ei) {
  float ar = re * p, ai = im * p;
  float mag = __builtin_amdgcn_exp2f(ar * 1.4426950408889634f);
  float n = __builtin_rintf(ai * 0.15915494309189535f);
  float r = __builtin_fmaf(-n, 6.2831854820251465f, ai);
  r = __builtin_fmaf(-n, -1.7484555314695172e-07f, r);
  float rev = r * 0.15915494309189535f;
  er = mag * __builtin_amdgcn_cosf(rev);
  ei = mag * __builtin_amdgcn_sinf(rev);
}

DI void ln_params(float sum, float m2, float& mb, float& sb) {
  mb = rbf(sum * (1.0f / D));
  float vb = rbf(m2 * (1.0f / D));
  sb = __builtin_amdgcn_rcpf(rbf(__builtin_amdgcn_sqrtf(rbf(vb + 1e-5f))));
}

constexpr int PREP_KGEN = 640, PREP_LN = 128, PREP_WQ = 100, PREP_WO = 100, PREP_WKV = 20, PREP_CS = 11, PREP_KV = 16;
constexpr int PREP_BLOCKS = PREP_KGEN + PREP_LN + PREP_WQ + PREP_WO + PREP_WKV + PREP_CS + PREP_KV;

DI void cexp_rev(float e0, float e1, float f0, float f1, float p, float& er, float& ei) {
  const float mag = __builtin_amdgcn_exp2f(__builtin_fmaf(e1, p, e0));
  const float rev = __builtin_amdgcn_fractf(__builtin_fmaf(f1, p, f0));
  er = mag * __builtin_amdgcn_cosf(rev);
  ei = mag * __builtin_amdgcn_sinf(rev);
}

__global__ __launch_bounds__(256) void prep_kernel(const u16* __restrict__ xu, const u16* __restrict__ lar,
                                                   const u16* __restrict__ aim, const u16* __restrict__ cre,
                                                   const u16* __restrict__ cim, const u16* __restrict__ ldt,
                                                   const u16* __restrict__ wq, const u16* __restrict__ wk,
                                                   const u16* __restrict__ wv, const u16* __restrict__ wo,
                                                   char* __restrict__ ws) {
  __shared__ __attribute__((aligned(16))) char smem[16 * 1296 + 64];
  const int tid = threadIdx.x, lane = tid & 63, w = tid >> 6;
  int bid = blockIdx.x;
  if (bid < PREP_KGEN) {
    const int d = bid;
    f32x4* prm = (f32x4*)smem;
    float* part = (float*)(smem + 2048);
    float* ktab = (float*)(smem + 2048 + 12288);
    if (tid < NS) {
      const float dt = rbf(expf(bf2f(ldt[d])));
      const float are = -rbf(expf(bf2f(lar[d * NS + tid])));
      const float ai = bf2f(aim[d * NS + tid]);
      const float cr = bf2f(cre[d * NS + tid]) * dt, ci = bf2f(cim[d * NS + tid]) * dt;
      const float mx = fmaxf(fabsf(cr), fabsf(ci));
      float lc = -1000.f, ph = 0.f;
      if (mx > 0.f) {
        const float a = cr / mx, b = ci / mx;
        lc = log2f(mx) + 0.5f * log2f(a * a + b * b);
        ph = atan2f(ci, cr) * 0.15915494309189535f;
      }
      f32x4 p = {are * dt * 1.4426950408889634f, ai * dt * 0.15915494309189535f, lc, ph};
      prm[tid] = p;
    }
    __syncthreads();
    const int i = lane & 15, kq = lane >> 4;
    const float A0 = (float)(128 * (i >> 3) + (i & 7));
    const float A1 = (float)(i < 8 ? 256 + 128 * (i >> 2) + 2 * (i & 3) : 512 + 128 * ((i - 8) >> 1) + 4 * (i & 1));
    const float A2 = (float)(i <= 8 ? 1024 + 128 * i : 0);
    const float Bp = (float)(8 * i);
    f32x4 acc0 = {}, acc1 = {}, acc2 = {};
#pragma unroll 2
    for (int t = 0; t < 8; ++t) {
      const f32x4 p = prm[32 * w + 4 * t + kq];
      float br, bi, ar, ai_;
      cexp_rev(0.f, p[0], 0.f, p[1], Bp, br, bi);
      cexp_rev(p[2], p[0], p[3], p[1], A0, ar, ai_);
      acc0 = __builtin_amdgcn_mfma_f32_16x16x4f32(ar, br, acc0, 0, 0, 0);
      acc0 = __builtin_amdgcn_mfma_f32_16x16x4f32(-ai_, bi, acc0, 0, 0, 0);
      cexp_rev(p[2], p[0], p[3], p[1], A1, ar, ai_);
      acc1 = __builtin_amdgcn_mfma_f32_16x16x4f32(ar, br, acc1, 0, 0, 0);
      acc1 = __builtin_amdgcn_mfma_f32_16x16x4f32(-ai_, bi, acc1, 0, 0, 0);
      cexp_rev(p[2], p[0], p[3], p[1], A2, ar, ai_);
      acc2 = __builtin_amdgcn_mfma_f32_16x16x4f32(ar, br, acc2, 0, 0, 0);
      acc2 = __builtin_amdgcn_mfma_f32_16x16x4f32(-ai_, bi, acc2, 0, 0, 0);
    }
#pragma unroll
    for (int r = 0; r < 4; ++r) {
      const int idx = (4 * kq + r) * 16 + i;
      part[(w * 3 + 0) * 256 + idx] = acc0[r];
      part[(w * 3 + 1) * 256 + idx] = acc1[r];
      part[(w * 3 + 2) * 256 + idx] = acc2[r];
    }
    __syncthreads();
    {
      const int r = tid >> 4, b8 = 8 * (tid & 15);
      const int p0 = 128 * (r >> 3) + (r & 7) + b8;
      const int p1 = (r < 8 ? 256 + 128 * (r >> 2) + 2 * (r & 3) : 512 + 128 * ((r - 8) >> 1) + 4 * (r & 1)) + b8;
      const int p2 = 1024 + 128 * r + b8;
      const float k0 = 2.f * (part[0 * 256 + tid] + part[3 * 256 + tid] + part[6 * 256 + tid] + part[9 * 256 + tid]);
      const float k1 = 2.f * (part[1 * 256 + tid] + part[4 * 256 + tid] + part[7 * 256 + tid] + part[10 * 256 + tid]);
      const float k2 = 2.f * (part[2 * 256 + tid] + part[5 * 256 + tid] + part[8 * 256 + tid] + part[11 * 256 + tid]);
      ktab[p0] = k0;
      ktab[r < 8 ? 128 + (p1 >> 1) : 256 + (p1 >> 2)] = k1;
      if (p2 <= 2048) ktab[384 + (p2 >> 3)] = k2;
    }
    __syncthreads();
    float v[8];
#pragma unroll
    for (int e = 0; e < 8; ++e) {
      const int l = 2047 - (8 * tid + e);
      const int lc = l < 1 ? 1 : l;
      const int sh = max(24 - __builtin_clz((unsigned)lc), 0);
      const int add = sh ? ((1 << sh) >> 1) - 1 + ((lc >> sh) & 1) : 0;
      const int idx = l < 1 ? 0 : 128 * sh + ((lc + add) >> sh);
      const float kv = ktab[idx];
      v[e] = l < 0 ? 0.f : kv;
    }
    u16* rgd = (u16*)(ws + WS_RG) + (size_t)d * RG_STRIDE;
    *(u32x4*)(rgd + 8 * tid) = u32x4{pk2(v[0], v[1]), pk2(v[2], v[3]), pk2(v[4], v[5]), pk2(v[6], v[7])};
    if (tid == 0) *(u32x4*)(rgd + 2048) = u32x4{0u, 0u, 0u, 0u};
    return;
  }
  bid -= PREP_KGEN;
  if (bid < PREP_LN) {
    const int l0 = bid * 16;
    const int row = tid >> 4, t16 = tid & 15;
    const u16* src = xu + (size_t)(l0 + row) * D;
    u32x4 v[5];
#pragma unroll
    for (int i = 0; i < 5; ++i) v[i] = *(const u32x4*)(src + (t16 + 16 * i) * 8);
    float s = 0.f;
#pragma unroll
    for (int i = 0; i < 5; ++i) {
#pragma unroll
      for (int j = 0; j < 4; ++j) s += bflo(v[i][j]) + bfhi(v[i][j]);
      *(u32x4*)(smem + row * 1296 + (t16 + 16 * i) * 16) = v[i];
    }
    s += __shfl_xor(s, 1); s += __shfl_xor(s, 2); s += __shfl_xor(s, 4); s += __shfl_xor(s, 8);
    const float mean = s * (1.0f / D);
    float m2 = 0.f;
#pragma unroll
    for (int i = 0; i < 5; ++i)
#pragma unroll
      for (int j = 0; j < 4; ++j) {
        float a = bflo(v[i][j]) - mean, b = bfhi(v[i][j]) - mean;
        m2 += a * a + b * b;
      }
    m2 += __shfl_xor(m2, 1); m2 += __shfl_xor(m2, 2); m2 += __shfl_xor(m2, 4); m2 += __shfl_xor(m2, 8);
    if (t16 == 0) {
      float mb, sb;
      ln_params(s, m2, mb, sb);
      f32x2 o = {mb, sb};
      *(f32x2*)(ws + WS_ST0 + (size_t)(l0 + row) * 8) = o;
    }
    __syncthreads();
    u16* xt = (u16*)(ws + WS_XT0);
    const int g = lane >> 4, q = (lane & 15) >> 2, p = lane & 3, i16 = lane & 15;
#pragma unroll
    for (int it = 0; it < 5; ++it) {
      const int cb = (it * 4 + w) * 2 + (g >> 1);
      const char* base = smem + (8 * (g & 1) + q) * 1296 + cb * 32 + 8 * p;
      s16x4 lo = tr_read(base);
      s16x4 hi = tr_read(base + 4 * 1296);
      u32x2 a = __builtin_bit_cast(u32x2, lo), b = __builtin_bit_cast(u32x2, hi);
      u32x4 o = {a[0], a[1], b[0], b[1]};
      *(u32x4*)(xt + (size_t)(cb * 16 + i16) * LT + l0 + 8 * (g & 1)) = o;
    }
    return;
  }
  bid -= PREP_LN;
  if (bid < PREP_WQ + PREP_WO) {
    const bool isq = bid < PREP_WQ;
    const int t = isq ? bid : bid - PREP_WQ;
    const int ks = t / 5, nb = t % 5;
    const u16* src = (isq ? wq : wo) + (size_t)(32 * ks) * D + 128 * nb;
#pragma unroll
    for (int i = 0; i < 2; ++i) {
      const int c = tid + 256 * i, r = c >> 4, cc = c & 15;
      *(u32x4*)(smem + r * 272 + cc * 16) = *(const u32x4*)(src + (size_t)r * D + cc * 8);
    }
    __syncthreads();
    const int g = lane >> 4, q = (lane & 15) >> 2, p = lane & 3;
#pragma unroll
    for (int i = 0; i < 2; ++i) {
      const int ntl = w * 2 + i;
      const char* base = smem + (8 * g + q) * 272 + ntl * 32 + 8 * p;
      s16x4 lo = tr_read(base);
      s16x4 hi = tr_read(base + 4 * 272);
      u32x2 a = __builtin_bit_cast(u32x2, lo), b = __builtin_bit_cast(u32x2, hi);
      u32x4 o = {a[0], a[1], b[0], b[1]};
      *(u32x4*)(ws + (isq ? WS_WQ : WS_WO) + (size_t)((8 * nb + ntl) * 20 + ks) * 1024 + lane * 16) = o;
    }
    return;
  }
  bid -= PREP_WQ + PREP_WO;
  if (bid < PREP_WKV) {
    const int ks = bid, nt = 40 + w;
    const int n = (lane & 15), k0 = 32 * ks + 8 * (lane >> 4);
    const u16* src = (w < 2) ? wk : wv;
    const int col = 16 * (w & 1) + n;
    const bool valid = col < DH;
    unsigned e[8];
#pragma unroll
    for (int j = 0; j < 8; ++j) e[j] = valid ? (unsigned)src[(size_t)(k0 + j) * DH + col] : 0u;
    u32x4 o = {e[0] | (e[1] << 16), e[2] | (e[3] << 16), e[4] | (e[5] << 16), e[6] | (e[7] << 16)};
    *(u32x4*)(ws + WS_WQ + (size_t)(nt * 20 + ks) * 1024 + lane * 16) = o;
    return;
  }
  bid -= PREP_WKV;
  if (bid < PREP_CS) {
    float* red = (float*)smem;
    if (bid < 10) {
      const int cg = tid & 7, kg = tid >> 3;
      const u16* src = wq + (size_t)(20 * kg) * D + 64 * bid + 8 * cg;
      float sm[8];
#pragma unroll
      for (int e = 0; e < 8; ++e) sm[e] = 0.f;
#pragma unroll
      for (int k = 0; k < 20; ++k) {
        const u32x4 v = *(const u32x4*)(src + (size_t)k * D);
#pragma unroll
        for (int j = 0; j < 4; ++j) { sm[2 * j] += bflo(v[j]); sm[2 * j + 1] += bfhi(v[j]); }
      }
#pragma unroll
      for (int e = 0; e < 8; ++e) red[kg * 64 + 8 * cg + e] = sm[e];
    } else {
      for (int c = tid; c < 32 * 64; c += 256) red[c] = 0.f;
      __syncthreads();
      for (int it = tid; it < 640; it += 256) {
        const int kg = it / 20, cp = it % 20;
        const u16* src = (cp < 10 ? wk : wv) + (size_t)(20 * kg) * DH + 2 * (cp % 10);
        float a = 0.f, b = 0.f;
#pragma unroll
        for (int k = 0; k < 20; ++k) {
          const unsigned v = *(const unsigned*)(src + (size_t)k * DH);
          a += bflo(v); b += bfhi(v);
        }
        const int col = (cp < 10 ? 0 : 32) + 2 * (cp % 10);
        red[kg * 64 + col] = a; red[kg * 64 + col + 1] = b;
      }
    }
    __syncthreads();
    if (tid < 64) {
      float t = 0.f;
#pragma unroll
      for (int kg = 0; kg < 32; ++kg) t += red[kg * 64 + tid];
      ((float*)(ws + WS_CS))[64 * bid + tid] = t;
    }
    return;
  }
  bid -= PREP_CS;
  {
    if (bid == 0) {
      for (int c = tid; c < 3 * 64 * 32; c += 256) ((unsigned*)(ws + WS_CNT))[c] = 0u;
    }
    for (int c = bid * 256 + tid; c < 64 * 256; c += PREP_KV * 256) {
      const int fr = (c >> 6) & 3, ln = c & 63;
      unsigned v = (fr >= 2 && (ln & 31) == DH) ? 0x3f803f80u : 0u;
      u32x4 o = {v, v, v, v};
      *(u32x4*)(ws + WS_KV + (size_t)c * 16) = o;
    }
  }
}

constexpr int CS = 4160;
__global__ __launch_bounds__(256) void conv_kernel(const u16* __restrict__ xt, const f32x4* __restrict__ stats,
                                                   const u16* __restrict__ rg, const u16* __restrict__ dskip,
                                                   u16* __restrict__ xout) {
  __shared__ __attribute__((aligned(16))) char smem[8 * CS + 96 * 80];
  char* KC = smem;
  char* U = smem + 8 * CS;
  const int tid = threadIdx.x, lane = tid & 63, w = tid >> 6;
  const int d = blockIdx.x;
  const u16* rgd = rg + (size_t)d * RG_STRIDE;
  const u32x4 xv = *(const u32x4*)(xt + (size_t)d * LT + 8 * tid);
  float mb[8], sb[8];
#pragma unroll
  for (int e = 0; e < 4; ++e) {
    const f32x4 s = stats[4 * tid + e];
    mb[2 * e] = s[0]; sb[2 * e] = s[1]; mb[2 * e + 1] = s[2]; sb[2 * e + 1] = s[3];
  }
  const u32x4 r0 = *(const u32x4*)(rgd + 8 * tid);
  const u32x4 r1 = *(const u32x4*)(rgd + 8 * tid + 8);
  const float dsk = bf2f(dskip[d]);
  {
    unsigned wv[8] = {r0[0], r0[1], r0[2], r0[3], r1[0], r1[1], r1[2], r1[3]};
#pragma unroll
    for (int s = 0; s < 8; ++s) {
      u32x4 o;
#pragma unroll
      for (int k = 0; k < 4; ++k) {
        if (s & 1) o[k] = __builtin_amdgcn_alignbit(wv[(s >> 1) + k + 1], wv[(s >> 1) + k], 16);
        else o[k] = wv[(s >> 1) + k];
      }
      *(u32x4*)(KC + s * CS + tid * 16) = o;
    }
    if (tid < 32) {
      u32x4 z = {0u, 0u, 0u, 0u};
      *(u32x4*)(KC + (tid >> 2) * CS + (256 + (tid & 3)) * 16) = z;
    }
  }
  float xf[8], uf[8];
#pragma unroll
  for (int j = 0; j < 4; ++j) { xf[2 * j] = bflo(xv[j]); xf[2 * j + 1] = bfhi(xv[j]); }
#pragma unroll
  for (int e = 0; e < 8; ++e) uf[e] = rbf(rbf(xf[e] - mb[e]) * sb[e]);
  {
    u32x4 o = {pk2(uf[0], uf[1]), pk2(uf[2], uf[3]), pk2(uf[4], uf[5]), pk2(uf[6], uf[7])};
    *(u32x4*)(U + ((tid >> 2) + 32) * 80 + (tid & 3) * 16) = o;
    if (tid < 128) {
      u32x4 z = {0u, 0u, 0u, 0u};
      *(u32x4*)(U + (tid >> 2) * 80 + (tid & 3) * 16) = z;
    }
  }
  __syncthreads();
  const int b = lane & 31, h = lane >> 5;
  const char* kbase = KC + (7 - (b & 7)) * CS + 16 * (255 - (b >> 3) + h);
  const char* ubase = U + (b + 32) * 80 + 16 * h;
  f32x16 acc0 = {}, acc1 = {};
  const int c0 = w == 0 ? 0 : (w == 1 ? 12 : 24);
  const int nboth = w < 2 ? 12 : (w == 2 ? 8 : 0);
  const int c1 = w == 3 ? 40 : 32, nsing = w < 2 ? 0 : (w == 2 ? 8 : 24);
  if (nboth) {
    const char* kp = kbase - 64 * c0;
    const char* up = ubase - 80 * c0;
    bf16x8 kf0 = *(const bf16x8*)(kp), kf1 = *(const bf16x8*)(kp + 32);
    bf16x8 a0 = *(const bf16x8*)(up), a1 = *(const bf16x8*)(up + 32);
    bf16x8 b0 = *(const bf16x8*)(up + 2560), b1 = *(const bf16x8*)(up + 2560 + 32);
    for (int c = 0; c < nboth; ++c) {
      kp -= 64; up -= 80;
      const bf16x8 nk0 = *(const bf16x8*)(kp), nk1 = *(const bf16x8*)(kp + 32);
      const bf16x8 na0 = *(const bf16x8*)(up), na1 = *(const bf16x8*)(up + 32);
      const bf16x8 nb0 = *(const bf16x8*)(up + 2560), nb1 = *(const bf16x8*)(up + 2560 + 32);
      acc0 = __builtin_amdgcn_mfma_f32_32x32x16_bf16(a0, kf0, acc0, 0, 0, 0);
      acc1 = __builtin_amdgcn_mfma_f32_32x32x16_bf16(b0, kf0, acc1, 0, 0, 0);
      acc0 = __builtin_amdgcn_mfma_f32_32x32x16_bf16(a1, kf1, acc0, 0, 0, 0);
      acc1 = __builtin_amdgcn_mfma_f32_32x32x16_bf16(b1, kf1, acc1, 0, 0, 0);
      kf0 = nk0; kf1 = nk1; a0 = na0; a1 = na1; b0 = nb0; b1 = nb1;
    }
  }
  if (nsing) {
    const char* kp = kbase - 64 * c1;
    const char* up = ubase + 80 * (32 - c1);
    bf16x8 kf0 = *(const bf16x8*)(kp), kf1 = *(const bf16x8*)(kp + 32);
    bf16x8 b0 = *(const bf16x8*)(up), b1 = *(const bf16x8*)(up + 32);
    for (int c = 0; c < nsing; ++c) {
      const bool more = c + 1 < nsing;
      kp -= more ? 64 : 0; up -= more ? 80 : 0;
      const bf16x8 nk0 = *(const bf16x8*)(kp), nk1 = *(const bf16x8*)(kp + 32);
      const bf16x8 nb0 = *(const bf16x8*)(up), nb1 = *(const bf16x8*)(up + 32);
      acc1 = __builtin_amdgcn_mfma_f32_32x32x16_bf16(b0, kf0, acc1, 0, 0, 0);
      acc1 = __builtin_amdgcn_mfma_f32_32x32x16_bf16(b1, kf1, acc1, 0, 0, 0);
      kf0 = nk0; kf1 = nk1; b0 = nb0; b1 = nb1;
    }
  }
  __syncthreads();
  float* part = (float*)smem;
#pragma unroll
  for (int r = 0; r < 16; ++r) {
    const int a = crow(r, h);
    part[w * 2048 + 32 * a + b] = acc0[r];
    part[w * 2048 + 1024 + 32 * a + b] = acc1[r];
  }
  __syncthreads();
  float y[8];
#pragma unroll
  for (int e = 0; e < 8; ++e) y[e] = 0.f;
#pragma unroll
  for (int ww = 0; ww < 4; ++ww) {
    const f32x4 p0 = *(const f32x4*)(part + ww * 2048 + 8 * tid);
    const f32x4 p1 = *(const f32x4*)(part + ww * 2048 + 8 * tid + 4);
#pragma unroll
    for (int e = 0; e < 4; ++e) { y[e] += p0[e]; y[4 + e] += p1[e]; }
  }
  float o[8];
#pragma unroll
  for (int e = 0; e < 8; ++e) o[e] = xf[e] + rbf(rbf(y[e]) + rbf(uf[e] * dsk));
  u32x4 ov = {pk2(o[0], o[1]), pk2(o[2], o[3]), pk2(o[4], o[5]), pk2(o[6], o[7])};
  *(u32x4*)(xout + (size_t)d * LT + 8 * tid) = ov;
}

__global__ __launch_bounds__(512, 2) void qkv_kernel(const u16* __restrict__ xt, const char* __restrict__ wf,
                                                     const float* __restrict__ csum, u16* __restrict__ Q,
                                                     char* __restrict__ kvf) {
  __shared__ __attribute__((aligned(16))) char smem[640 * 128 + 512 + 6 * 5 * 1024];
  f32x2* lnp = (f32x2*)(smem + 640 * 128);
  const int tid = threadIdx.x, lane = tid & 63, w = tid >> 6;
  const int idx = blockIdx.x >> 3;
  const int rt = (idx >> 3) * 8 + (blockIdx.x & 7), ns = idx & 7;
  const int l0 = 64 * rt;
  {
    const int dr = lane >> 3, c = lane & 7;
    const int sw = ((dr >> 1) & 1) | ((w & 1) << 1);
#pragma unroll
    for (int i = 0; i < 10; ++i) {
      const int piece = i * 8 + w;
      const int d = 8 * piece + dr;
      const u16* src = xt + (size_t)d * LT + l0 + 8 * (c ^ (sw << 1));
      __builtin_amdgcn_global_load_lds((const __attribute__((address_space(1))) void*)src,
                                       (__attribute__((address_space(3))) void*)(smem + piece * 1024), 16, 0, 0);
    }
  }
  const int P = w % 3, Hh = w / 3;
  const int ntp = ns * 6 + 2 * P;
  const bool active = (w < 6) && (ntp < 44);
  const char* wbase = wf + (size_t)((active ? ntp : 0) * 20 + 10 * Hh) * 1024 + lane * 16;
  bf16x8 w0[10], w1[10];
  const f32x4 accc = *(const f32x4*)(csum + 16 * ((active ? ntp : 0) + Hh % 2) + 4 * (lane >> 4));
  if (active) {
#pragma unroll
    for (int ks = 0; ks < 10; ++ks) w0[ks] = *(const bf16x8*)(wbase + ks * 1024);
#pragma unroll
    for (int ks = 0; ks < 10; ++ks) w1[ks] = *(const bf16x8*)(wbase + 20 * 1024 + ks * 1024);
  }
  __syncthreads();
  const int g = lane >> 4, q = (lane & 15) >> 2, p = lane & 3;
  const int sw = ((q >> 1) & 1) | ((g & 1) << 1);
  const char* ub = smem + (8 * g + q) * 128 + 8 * p;
  const u32x4 onesu = {0x3f803f80u, 0x3f803f80u, 0x3f803f80u, 0x3f803f80u};
  const bf16x8 ones = __builtin_bit_cast(bf16x8, onesu);
  f32x4 acc[4];
  f32x4* xch = (f32x4*)(smem + 640 * 128 + 512);
#pragma unroll
  for (int a = 0; a < 4; ++a) acc[a] = f32x4{};
  if (active) {
    f32x4 bcc[4];
#pragma unroll
    for (int a = 0; a < 4; ++a) bcc[a] = f32x4{};
    const char* ubh = ub + Hh * 10 * 4096;
    bf16x8 uf[2][8];
    auto rd2 = [&](int kp, bf16x8* dst) {
#pragma unroll
      for (int kk = 0; kk < 2; ++kk)
#pragma unroll
        for (int lt = 0; lt < 4; ++lt) {
          const char* pp = ubh + (2 * kp + kk) * 4096 + ((lt ^ sw) << 5);
          s16x4 a0 = tr_read(pp), a1 = tr_read(pp + 512);
          dst[kk * 4 + lt] = __builtin_bit_cast(bf16x8, __builtin_shufflevector(a0, a1, 0, 1, 2, 3, 4, 5, 6, 7));
        }
    };
    rd2(0, uf[0]);
#pragma unroll
    for (int kp = 0; kp < 5; ++kp) {
      __builtin_amdgcn_sched_barrier(0);
      if (kp + 1 < 5) rd2(kp + 1, uf[(kp + 1) & 1]);
      __builtin_amdgcn_sched_barrier(0);
#pragma unroll
      for (int kk = 0; kk < 2; ++kk) {
        const int ks = 2 * kp + kk;
#pragma unroll
        for (int lt = 0; lt < 4; ++lt) {
          acc[lt] = __builtin_amdgcn_mfma_f32_16x16x32_bf16(w0[ks], uf[kp & 1][kk * 4 + lt], acc[lt], 0, 0, 0);
          bcc[lt] = __builtin_amdgcn_mfma_f32_16x16x32_bf16(w1[ks], uf[kp & 1][kk * 4 + lt], bcc[lt], 0, 0, 0);
        }
      }
    }
    if (Hh) {
#pragma unroll
      for (int a = 0; a < 4; ++a) { xch[(w * 5 + a) * 64 + lane] = acc[a]; acc[a] = bcc[a]; }
    } else {
#pragma unroll
      for (int a = 0; a < 4; ++a) xch[(w * 5 + a) * 64 + lane] = bcc[a];
    }
  } else if (w >= 6) {
    bf16x8 uf[2][8];
    auto rd4 = [&](int kp, bf16x8* dst) {
#pragma unroll
      for (int kk = 0; kk < 4; ++kk)
#pragma unroll
        for (int j = 0; j < 2; ++j) {
          const char* pp = ub + (4 * kp + kk) * 4096 + (((2 * (w - 6) + j) ^ sw) << 5);
          s16x4 a0 = tr_read(pp), a1 = tr_read(pp + 512);
          dst[kk * 2 + j] = __builtin_bit_cast(bf16x8, __builtin_shufflevector(a0, a1, 0, 1, 2, 3, 4, 5, 6, 7));
        }
    };
    rd4(0, uf[0]);
#pragma unroll
    for (int kp = 0; kp < 5; ++kp) {
      __builtin_amdgcn_sched_barrier(0);
      if (kp + 1 < 5) rd4(kp + 1, uf[(kp + 1) & 1]);
      __builtin_amdgcn_sched_barrier(0);
#pragma unroll
      for (int kk = 0; kk < 4; ++kk)
#pragma unroll
        for (int j = 0; j < 2; ++j) {
          const bf16x8 u0 = uf[kp & 1][kk * 2 + j];
          acc[j] = __builtin_amdgcn_mfma_f32_16x16x32_bf16(ones, u0, acc[j], 0, 0, 0);
          acc[2 + j] = __builtin_amdgcn_mfma_f32_16x16x32_bf16(u0, u0, acc[2 + j], 0, 0, 0);
        }
    }
    const int li = lane & 15;
    if (g == (li >> 2)) {
#pragma unroll
      for (int j = 0; j < 2; ++j) {
        const float sx = acc[j][0];
        const f32x4 gm = acc[2 + j];
        const int r = li & 3;
        const float sxx = r == 0 ? gm[0] : (r == 1 ? gm[1] : (r == 2 ? gm[2] : gm[3]));
        float mb, sb;
        ln_params(sx, fmaxf(sxx - sx * sx * (1.0f / D), 0.f), mb, sb);
        f32x2 o = {mb, sb};
        lnp[16 * (2 * (w - 6) + j) + li] = o;
      }
    }
  }
  __syncthreads();
  if (!active) return;
  const int nt = ntp + Hh;
  {
    const int pw = Hh ? w - 3 : w + 3;
#pragma unroll
    for (int a = 0; a < 4; ++a) acc[a] += xch[(pw * 5 + a) * 64 + lane];
  }
#pragma unroll
  for (int lt = 0; lt < 4; ++lt) {
    const f32x2 pr = lnp[16 * lt + (lane & 15)];
#pragma unroll
    for (int e = 0; e < 4; ++e) acc[lt][e] = (acc[lt][e] - pr[0] * accc[e]) * pr[1];
  }
  const float qscale = 0.22360679774997896f * 1.4426950408889634f;
#pragma unroll
  for (int lt = 0; lt < 4; ++lt) {
    const int l = l0 + 16 * lt + (lane & 15);
    const f32x4 r = acc[lt];
    if (nt < 40) {
      u32x2 o = {pk2(r[0] * qscale, r[1] * qscale), pk2(r[2] * qscale, r[3] * qscale)};
      *(u32x2*)(Q + (size_t)l * D + 16 * nt + 4 * g) = o;
    } else if (nt < 42) {
      const int r32 = l & 31;
      u32x2 o = {pk2(r[0], r[1]), pk2(r[2], r[3])};
      *(u32x2*)(kvf + (size_t)(l >> 5) * 4096 + (nt - 40) * 1024 + (r32 + 32 * (g >> 1)) * 16 + 8 * (g & 1)) = o;
    } else {
      const int r32 = l & 31;
      const int s_ = r32 >> 4, jh = (r32 >> 3) & 1, hh = (r32 >> 2) & 1, jl = r32 & 3;
      const int j = 4 * jh + jl;
#pragma unroll
      for (int e = 0; e < 4; ++e) {
        const int dv = 16 * (nt - 42) + 4 * g + e;
        if (dv < DH) {
          u16 bv = (u16)(pk2(r[e], 0.f) & 0xffffu);
          *(u16*)(kvf + (size_t)(l >> 5) * 4096 + (2 + s_) * 1024 + (dv + 32 * hh) * 16 + 2 * j) = bv;
        }
      }
    }
  }
}

constexpr int ACH = 8;
constexpr int WLK = 9;
DI void attn_pack(const f32x16& s, float delta, u32x4& pa, u32x4& pb) {
#pragma unroll
  for (int r = 0; r < 4; ++r) {
    pa[r] = pk2(__builtin_amdgcn_exp2f(s[2 * r] - delta), __builtin_amdgcn_exp2f(s[2 * r + 1] - delta));
    pb[r] = pk2(__builtin_amdgcn_exp2f(s[8 + 2 * r] - delta), __builtin_amdgcn_exp2f(s[8 + 2 * r + 1] - delta));
  }
}
DI void attn_safe_tile(const char* tb, const bf16x8& qf0, const bf16x8& qf1, f32x16& o, f32x16& negm, bool first) {
  const bf16x8 k0 = *(const bf16x8*)(tb);
  const bf16x8 k1 = *(const bf16x8*)(tb + 1024);
  const bf16x8 v0 = *(const bf16x8*)(tb + 2048);
  const bf16x8 v1 = *(const bf16x8*)(tb + 3072);
  f32x16 s = __builtin_amdgcn_mfma_f32_32x32x16_bf16(k0, qf0, negm, 0, 0, 0);
  s = __builtin_amdgcn_mfma_f32_32x32x16_bf16(k1, qf1, s, 0, 0, 0);
  float tm = s[0];
#pragma unroll
  for (int r = 1; r < 16; ++r) tm = fmaxf(tm, s[r]);
  tm = fmaxf(tm, __shfl_xor(tm, 32));
  const float delta = first ? tm : fmaxf(tm, 0.f);
  const float sc = first ? 0.f : __builtin_amdgcn_exp2f(-delta);
#pragma unroll
  for (int r = 0; r < 16; ++r) { o[r] *= sc; negm[r] -= delta; }
  u32x4 pa, pb;
  attn_pack(s, delta, pa, pb);
  o = __builtin_amdgcn_mfma_f32_32x32x16_bf16(v0, __builtin_bit_cast(bf16x8, pa), o, 0, 0, 0);
  o = __builtin_amdgcn_mfma_f32_32x32x16_bf16(v1, __builtin_bit_cast(bf16x8, pb), o, 0, 0, 0);
}
#define SBAR() __builtin_amdgcn_sched_barrier(0)
#define EXP4(P, I, S, B)                                                                   \
  P[I] = pk2(__builtin_amdgcn_exp2f(S[B]), __builtin_amdgcn_exp2f(S[B + 1]));              \
  P[I + 1] = pk2(__builtin_amdgcn_exp2f(S[B + 2]), __builtin_amdgcn_exp2f(S[B + 3]));
struct AttnState {
  f32x16 o, s_cur;
  bf16x8 kA0, kA1;
  bf16x8 vA0, vA1;
  bf16x8 vB0, vB1;
  bf16x8 v1p;
  u32x4 pbp;
};
template <bool FIRST, bool LAST, typename F>
DI void attn_chunk(AttnState& st, const char* cur, const char* nxt, const bf16x8& qf0, const bf16x8& qf1,
                   const f32x16& negm, F sync) {
  f32x16 s_nxt;
  bf16x8 kB0, kB1, vC0, vC1;
  u32x4 pa, pb;
#pragma unroll
  for (int i = 0; i < ACH; ++i) {
    const bool has1 = !(LAST && i + 1 >= ACH);
    const bool has2 = !(LAST && i + 2 >= ACH);
    SBAR();
    if (!LAST && i == ACH - 2) sync();
    if (has1) s_nxt = __builtin_amdgcn_mfma_f32_32x32x16_bf16(st.kA0, qf0, negm, 0, 0, 0);
    if (has2) {
      const char* tp = (i + 2 < ACH) ? cur + (i + 2) * 4096 : nxt + (i + 2 - ACH) * 4096;
      kB0 = *(const bf16x8*)(tp);
      kB1 = *(const bf16x8*)(tp + 1024);
      vC0 = *(const bf16x8*)(tp + 2048);
      vC1 = *(const bf16x8*)(tp + 3072);
    }
    SBAR();
    EXP4(pa, 0, st.s_cur, 0);
    SBAR();
    if (!(FIRST && i == 0)) st.o = __builtin_amdgcn_mfma_f32_32x32x16_bf16(st.v1p, __builtin_bit_cast(bf16x8, st.pbp), st.o, 0, 0, 0);
    SBAR();
    EXP4(pa, 2, st.s_cur, 4);
    SBAR();
    if (has1) s_nxt = __builtin_amdgcn_mfma_f32_32x32x16_bf16(st.kA1, qf1, s_nxt, 0, 0, 0);
    SBAR();
    EXP4(pb, 0, st.s_cur, 8);
    SBAR();
    st.o = __builtin_amdgcn_mfma_f32_32x32x16_bf16(st.vA0, __builtin_bit_cast(bf16x8, pa), st.o, 0, 0, 0);
    SBAR();
    EXP4(pb, 2, st.s_cur, 12);
    st.pbp = pb; st.v1p = st.vA1;
    st.s_cur = s_nxt; st.kA0 = kB0; st.kA1 = kB1;
    st.vA0 = st.vB0; st.vA1 = st.vB1; st.vB0 = vC0; st.vB1 = vC1;
  }
  SBAR();
  if (LAST) st.o = __builtin_amdgcn_mfma_f32_32x32x16_bf16(st.v1p, __builtin_bit_cast(bf16x8, st.pbp), st.o, 0, 0, 0);
}

typedef __attribute__((address_space(1))) unsigned gu32;
typedef __attribute__((address_space(1))) unsigned long long gu64;
#define RLX_AGENT __ATOMIC_RELAXED, __HIP_MEMORY_SCOPE_AGENT

template <bool FINAL>
__global__ __launch_bounds__(512, 2) void attn_kernel(const u16* __restrict__ Q, const char* __restrict__ kvf,
                                                      u16* O, const char* __restrict__ wf,
                                                      const u16* __restrict__ xres, u16* __restrict__ xout,
                                                      f32x2* pstats, f32x2* __restrict__ fstats, unsigned* cnt) {
  __shared__ __attribute__((aligned(16))) char smem[2 * ACH * 4096 + 10 * WLK * 1024];
  const int tid = threadIdx.x, lane = tid & 63, w = tid >> 6;
  const int hq = lane & 31, h = lane >> 5;
  const int idx = blockIdx.x >> 3;
  const int rt = (idx >> 2) * 8 + (blockIdx.x & 7), ns = idx & 3;
  const int row = 32 * rt + 8 * ns + w;
  auto stage = [&](int chunk, int buf) {
#pragma unroll
    for (int i = 0; i < ACH / 2; ++i) {
      const char* src = kvf + (size_t)chunk * ACH * 4096 + w * (ACH * 512) + i * 1024 + lane * 16;
      __builtin_amdgcn_global_load_lds((const __attribute__((address_space(1))) void*)src,
                                       (__attribute__((address_space(3))) void*)(smem + buf * ACH * 4096 + w * (ACH * 512) + i * 1024),
                                       16, 0, 0);
    }
  };
  stage(0, 0);
  bf16x8 qf0, qf1;
  {
    const u16* qp = Q + (size_t)row * D + hq * DH;
    u32x2 a = *(const u32x2*)(qp + 8 * h), b = *(const u32x2*)(qp + 8 * h + 4);
    u32x4 t = {a[0], a[1], b[0], b[1]};
    qf0 = __builtin_bit_cast(bf16x8, t);
    u32x2 c = *(const u32x2*)(qp + 16);
    u32x4 t1 = {h ? 0u : c[0], h ? 0u : c[1], 0u, 0u};
    qf1 = __builtin_bit_cast(bf16x8, t1);
  }
  stage(1, 1);
  f32x16 negm = {};
  const int l0 = 32 * rt;
  const int g = lane >> 4, i16 = lane & 15;
  const int nt0 = ns * 10 + 2 * (w < 5 ? w : 0);
  bf16x8 w0[20 - WLK], w1[20 - WLK];
  u32x2 xr[2][2];
  u16 xs[2][2][4];
  asm volatile("s_waitcnt vmcnt(%0)" ::"i"(ACH / 2) : "memory");
  __builtin_amdgcn_s_barrier();
  asm volatile("" ::: "memory");
  const char* buf0 = smem + lane * 16;
  const char* buf1 = smem + ACH * 4096 + lane * 16;
  AttnState st;
  {
    const bf16x8 k0 = *(const bf16x8*)(buf0);
    const bf16x8 k1 = *(const bf16x8*)(buf0 + 1024);
    st.vA0 = *(const bf16x8*)(buf0 + 2048);
    st.vA1 = *(const bf16x8*)(buf0 + 3072);
    st.kA0 = *(const bf16x8*)(buf0 + 4096);
    st.kA1 = *(const bf16x8*)(buf0 + 4096 + 1024);
    st.vB0 = *(const bf16x8*)(buf0 + 4096 + 2048);
    st.vB1 = *(const bf16x8*)(buf0 + 4096 + 3072);
    f32x16 s = __builtin_amdgcn_mfma_f32_32x32x16_bf16(k0, qf0, negm, 0, 0, 0);
    s = __builtin_amdgcn_mfma_f32_32x32x16_bf16(k1, qf1, s, 0, 0, 0);
    float tm = s[0];
#pragma unroll
    for (int r = 1; r < 16; ++r) tm = fmaxf(tm, s[r]);
    tm = fmaxf(tm, __shfl_xor(tm, 32));
#pragma unroll
    for (int r = 0; r < 16; ++r) { st.s_cur[r] = s[r] - tm; negm[r] = -tm; st.o[r] = 0.f; }
    st.v1p = st.vA1; st.pbp = u32x4{0u, 0u, 0u, 0u};
  }
  const char* wbase = wf + (size_t)(nt0 * 20 + WLK) * 1024 + lane * 16;
  auto sync_stage = [&](int ch) {
    asm volatile("s_waitcnt vmcnt(0) lgkmcnt(0)" ::: "memory");
    __builtin_amdgcn_s_barrier();
    asm volatile("" ::: "memory");
    if (ch + 2 < 64 / ACH) stage(ch + 2, ch & 1);
    if (ch < 5) {
      for (int f = ch * 2 * WLK + w; f < (ch + 1) * 2 * WLK; f += 8) {
        const int ntl = f / WLK, ks = f % WLK;
        const char* src = wf + (size_t)((ns * 10 + ntl) * 20 + ks) * 1024 + lane * 16;
        __builtin_amdgcn_global_load_lds((const __attribute__((address_space(1))) void*)src,
                                         (__attribute__((address_space(3))) void*)(smem + 2 * ACH * 4096 + f * 1024), 16, 0, 0);
      }
    }
  };
  attn_chunk<true, false>(st, buf0, buf1, qf0, qf1, negm, [&]() { sync_stage(0); });
  for (int ch = 1; ch < 64 / ACH - 3; ++ch) {
    const char* cur = (ch & 1) ? buf1 : buf0;
    const char* nxt = (ch & 1) ? buf0 : buf1;
    attn_chunk<false, false>(st, cur, nxt, qf0, qf1, negm, [&]() { sync_stage(ch); });
  }
  attn_chunk<false, false>(st, buf1, buf0, qf0, qf1, negm, [&]() {
    sync_stage(5);
    if (w < 5) {
#pragma unroll
      for (int ks = 0; ks < 20 - WLK; ++ks) w0[ks] = *(const bf16x8*)(wbase + ks * 1024);
    }
  });
  attn_chunk<false, false>(st, buf0, buf1, qf0, qf1, negm, [&]() {
    sync_stage(6);
    if (w < 5) {
#pragma unroll
      for (int ks = 0; ks < 20 - WLK; ++ks) w1[ks] = *(const bf16x8*)(wbase + 20 * 1024 + ks * 1024);
#pragma unroll
      for (int a = 0; a < 2; ++a)
#pragma unroll
        for (int lt = 0; lt < 2; ++lt) {
          if (FINAL) {
#pragma unroll
            for (int e = 0; e < 4; ++e) xs[a][lt][e] = xres[(size_t)(16 * (nt0 + a) + 4 * g + e) * LT + l0 + 16 * lt + i16];
          } else {
            xr[a][lt] = *(const u32x2*)(xres + (size_t)(16 * (nt0 + a) + i16) * LT + l0 + 16 * lt + 4 * g);
          }
        }
    }
  });
  attn_chunk<false, true>(st, buf1, buf0, qf0, qf1, negm, [&]() {});
  f32x16 o = st.o;
  {
    float amax = 0.f;
#pragma unroll
    for (int r = 0; r < 12; ++r) amax = fmaxf(amax, fabsf(o[r]));
    if (__builtin_expect(__any(!(amax < 1.0e30f)), 0)) {
      o = f32x16{};
      negm = f32x16{};
      for (int t = 0; t < 64; ++t) attn_safe_tile(kvf + (size_t)t * 4096 + lane * 16, qf0, qf1, o, negm, t == 0);
    }
  }
  __syncthreads();
  {
    float lsum = o[8];
    lsum = __shfl(lsum, hq + 32);
    const float inv = 1.0f / lsum;
#pragma unroll
    for (int g = 0; g < 3; ++g) {
      const int dv = 8 * g + 4 * h;
      if (dv < DH) {
        const int k = hq * DH + dv;
        u32x2 ov = {pk2(o[4 * g] * inv, o[4 * g + 1] * inv), pk2(o[4 * g + 2] * inv, o[4 * g + 3] * inv)};
        *(u32x2*)(smem + ((k >> 3) * 8 + w) * 16 + (k & 7) * 2) = ov;
      }
    }
    __syncthreads();
    const __amdgpu_buffer_rsrc_t rs = __builtin_amdgcn_make_buffer_rsrc((void*)O, 0, L * D * 2, 0x00020000);
    const unsigned obase = (unsigned)((row >> 4) * 20480 + (8 * (ns & 1)) * 16);
#pragma unroll
    for (int i = 0; i < 2; ++i) {
      const int c = tid + 512 * i;
      if (c < 640) {
        const u32x4 t = *(const u32x4*)(smem + c * 16);
        __builtin_amdgcn_raw_buffer_store_b128(t, rs, obase + (unsigned)((c >> 3) * 256 + (c & 7) * 16), 0, 16);
      }
    }
  }
  asm volatile("" ::: "memory");
  asm volatile("s_waitcnt vmcnt(0)" ::: "memory");
  __builtin_amdgcn_s_barrier();
  gu32* c0 = (gu32*)cnt + rt * 32;
  if (tid == 448) {
    __hip_atomic_fetch_add(c0, 1u, RLX_AGENT);
    for (unsigned spins = 0; spins < (1u << 22); ++spins) {
      if (__hip_atomic_load(c0, RLX_AGENT) >= 4u) break;
      __builtin_amdgcn_s_sleep(1);
    }
  }
  asm volatile("" ::: "memory");
  __builtin_amdgcn_s_barrier();
  asm volatile("" ::: "memory");
  {
    const __amdgpu_buffer_rsrc_t rs = __builtin_amdgcn_make_buffer_rsrc((void*)O, 0, L * D * 2, 0x00020000);
    u32x4 t[5];
#pragma unroll
    for (int i = 0; i < 5; ++i)
      t[i] = __builtin_amdgcn_raw_buffer_load_b128(rs, (unsigned)(rt * 40960 + (w * 5 + i) * 1024 + lane * 16), 0, 16);
#pragma unroll
    for (int i = 0; i < 5; ++i) *(u32x4*)(smem + (w * 5 + i) * 1024 + lane * 16) = t[i];
  }
  __syncthreads();
  f32x4 acc[2][2];
  acc[0][0] = f32x4{}; acc[0][1] = f32x4{}; acc[1][0] = f32x4{}; acc[1][1] = f32x4{};
  float* stt = (float*)(smem + 40960);
  if (w < 5) {
    const char* obase = smem + lane * 16;
    const char* wl = smem + 2 * ACH * 4096 + (2 * w) * WLK * 1024 + lane * 16;
#pragma unroll
    for (int ks = 0; ks < 20; ++ks) {
      const bf16x8 o0 = *(const bf16x8*)(obase + ks * 1024);
      const bf16x8 o1 = *(const bf16x8*)(obase + 20 * 1024 + ks * 1024);
      const bf16x8 wa = ks < WLK ? *(const bf16x8*)(wl + ks * 1024) : w0[ks < WLK ? 0 : ks - WLK];
      const bf16x8 wb = ks < WLK ? *(const bf16x8*)(wl + (WLK + ks) * 1024) : w1[ks < WLK ? 0 : ks - WLK];
      if (FINAL) {
        acc[0][0] = __builtin_amdgcn_mfma_f32_16x16x32_bf16(wa, o0, acc[0][0], 0, 0, 0);
        acc[0][1] = __builtin_amdgcn_mfma_f32_16x16x32_bf16(wa, o1, acc[0][1], 0, 0, 0);
        acc[1][0] = __builtin_amdgcn_mfma_f32_16x16x32_bf16(wb, o0, acc[1][0], 0, 0, 0);
        acc[1][1] = __builtin_amdgcn_mfma_f32_16x16x32_bf16(wb, o1, acc[1][1], 0, 0, 0);
      } else {
        acc[0][0] = __builtin_amdgcn_mfma_f32_16x16x32_bf16(o0, wa, acc[0][0], 0, 0, 0);
        acc[0][1] = __builtin_amdgcn_mfma_f32_16x16x32_bf16(o1, wa, acc[0][1], 0, 0, 0);
        acc[1][0] = __builtin_amdgcn_mfma_f32_16x16x32_bf16(o0, wb, acc[1][0], 0, 0, 0);
        acc[1][1] = __builtin_amdgcn_mfma_f32_16x16x32_bf16(o1, wb, acc[1][1], 0, 0, 0);
      }
    }
    if (FINAL) {
#pragma unroll
      for (int a = 0; a < 2; ++a)
#pragma unroll
        for (int lt = 0; lt < 2; ++lt) {
          const int l = l0 + 16 * lt + i16;
          const int n = 16 * (nt0 + a) + 4 * g;
          float r[4];
#pragma unroll
          for (int e = 0; e < 4; ++e) r[e] = bf2f(xs[a][lt][e]) + rbf(acc[a][lt][e]);
          u32x2 ov = {pk2(r[0], r[1]), pk2(r[2], r[3])};
          *(u32x2*)(xout + (size_t)l * D + n) = ov;
        }
    } else {
#pragma unroll
      for (int a = 0; a < 2; ++a)
#pragma unroll
        for (int lt = 0; lt < 2; ++lt) {
          const int n = 16 * (nt0 + a) + i16;
          const int l = l0 + 16 * lt + 4 * g;
          float r[4];
          r[0] = rbf(bflo(xr[a][lt][0]) + rbf(acc[a][lt][0]));
          r[1] = rbf(bfhi(xr[a][lt][0]) + rbf(acc[a][lt][1]));
          r[2] = rbf(bflo(xr[a][lt][1]) + rbf(acc[a][lt][2]));
          r[3] = rbf(bfhi(xr[a][lt][1]) + rbf(acc[a][lt][3]));
          u32x2 ov = {pk2(r[0], r[1]), pk2(r[2], r[3])};
          *(u32x2*)(xout + (size_t)n * LT + l) = ov;
          const int nl = 16 * (2 * w + a) + i16;
#pragma unroll
          for (int e = 0; e < 4; ++e) stt[(16 * lt + 4 * g + e) * 164 + nl] = r[e];
        }
    }
  }
  if (!FINAL) {
    __syncthreads();
    unsigned* flag = (unsigned*)(smem + 40960 + 32 * 164 * 4);
    if (tid < 256) {
      const int l = tid >> 3, p8 = tid & 7;
      float v[20];
      float s = 0.f;
#pragma unroll
      for (int e = 0; e < 20; ++e) { v[e] = stt[l * 164 + p8 + 8 * e]; s += v[e]; }
      s += __shfl_xor(s, 1); s += __shfl_xor(s, 2); s += __shfl_xor(s, 4);
      const float mean = s * (1.0f / 160.f);
      float m2 = 0.f;
#pragma unroll
      for (int e = 0; e < 20; ++e) { float a = v[e] - mean; m2 += a * a; }
      m2 += __shfl_xor(m2, 1); m2 += __shfl_xor(m2, 2); m2 += __shfl_xor(m2, 4);
      if (p8 == 0) {
        const unsigned long long pv = (unsigned long long)__float_as_uint(s) | ((unsigned long long)__float_as_uint(m2) << 32);
        __hip_atomic_store((gu64*)(pstats + (size_t)(l0 + l) * 4 + ns), pv, RLX_AGENT);
      }
      asm volatile("s_waitcnt vmcnt(0)" ::: "memory");
    }
    __syncthreads();
    if (tid == 0) *flag = __hip_atomic_fetch_add((gu32*)cnt + (64 + rt) * 32, 1u, RLX_AGENT);
    __syncthreads();
    if (*flag == 3u && tid < 32) {
      float ps[4], pm[4];
      float sum = 0.f;
#pragma unroll
      for (int p = 0; p < 4; ++p) {
        const unsigned long long pv = __hip_atomic_load((gu64*)(pstats + (size_t)(l0 + tid) * 4 + p), RLX_AGENT);
        ps[p] = __uint_as_float((unsigned)pv); pm[p] = __uint_as_float((unsigned)(pv >> 32));
        sum += ps[p];
      }
      const float mean = sum * (1.0f / D);
      float m2 = 0.f;
#pragma unroll
      for (int p = 0; p < 4; ++p) {
        const float dm = ps[p] * (4.0f / D) - mean;
        m2 += pm[p] + dm * dm * (D / 4.0f);
      }
      float mb, sb;
      ln_params(sum, m2, mb, sb);
      f32x2 ov = {mb, sb};
      fstats[l0 + tid] = ov;
    }
  }
}

extern "C" void kernel_launch(void* const* d_in, const int* in_sizes, int n_in, void* d_out, int out_size,
                              void* d_ws, size_t ws_size, hipStream_t stream) {
  const u16* xu = (const u16*)d_in[0];
  const u16* lar = (const u16*)d_in[1];
  const u16* aim = (const u16*)d_in[2];
  const u16* cre = (const u16*)d_in[3];
  const u16* cim = (const u16*)d_in[4];
  const u16* ldt = (const u16*)d_in[5];
  const u16* dsk = (const u16*)d_in[6];
  const u16* wq = (const u16*)d_in[8];
  const u16* wk = (const u16*)d_in[9];
  const u16* wv = (const u16*)d_in[10];
  const u16* wo = (const u16*)d_in[11];
  char* ws = (char*)d_ws;
  u16* out = (u16*)d_out;
  u16* rg = (u16*)(ws + WS_RG);
  u16* xt0 = (u16*)(ws + WS_XT0);
  u16* xt1 = (u16*)(ws + WS_XT1);
  u16* xt2 = (u16*)(ws + WS_XT2);
  u16* xt3 = (u16*)(ws + WS_XT3);
  f32x2* st0 = (f32x2*)(ws + WS_ST0);
  f32x2* st2 = (f32x2*)(ws + WS_ST2);
  u16* Q = (u16*)(ws + WS_Q);
  u16* O = (u16*)(ws + WS_O);
  char* kvf = ws + WS_KV;

  f32x2* sf2 = (f32x2*)(ws + WS_SF2);
  unsigned* cnt = (unsigned*)(ws + WS_CNT);
  prep_kernel<<<PREP_BLOCKS, 256, 0, stream>>>(xu, lar, aim, cre, cim, ldt, wq, wk, wv, wo, ws);
  conv_kernel<<<D, 256, 0, stream>>>(xt0, (const f32x4*)st0, rg, dsk, xt1);
  qkv_kernel<<<256, 512, 0, stream>>>(xt1, ws + WS_WQ, (const float*)(ws + WS_CS), Q, kvf);
  attn_kernel<false><<<256, 512, 0, stream>>>(Q, kvf, O, ws + WS_WO, xt1, xt2, st2, sf2, cnt);
  conv_kernel<<<D, 256, 0, stream>>>(xt2, (const f32x4*)sf2, rg, dsk, xt3);
  qkv_kernel<<<256, 512, 0, stream>>>(xt3, ws + WS_WQ, (const float*)(ws + WS_CS), Q, kvf);
  attn_kernel<true><<<256, 512, 0, stream>>>(Q, kvf, O, ws + WS_WO, xt3, out, st2, sf2, cnt + 128 * 32);
}
```

```cpp
#include <hip/hip_runtime.h>
#include <hip/hip_bf16.h>
#include <stdint.h>

typedef __attribute__((ext_vector_type(8))) __bf16 bf16x8;
typedef __attribute__((ext_vector_type(2))) __bf16 bf16x2;
typedef __attribute__((ext_vector_type(4))) short s16x4;
typedef __attribute__((ext_vector_type(16))) float f32x16;
typedef __attribute__((ext_vector_type(4))) float f32x4;
typedef __attribute__((ext_vector_type(2))) float f32x2;
typedef __attribute__((ext_vector_type(4))) unsigned u32x4;
typedef __attribute__((ext_vector_type(2))) unsigned u32x2;
typedef unsigned short u16;
#define DI __device__ __forceinline__

constexpr int L = 2048, D = 640, NS = 128, H = 32, DH = 20;
constexpr int TS = 2624, TK1 = 1024, TV = 1280, TVB = 336;
constexpr int RG_STRIDE = 2112;

constexpr int LT = 2112;
constexpr size_t SZ_XT = (size_t)D * LT * 2;
constexpr size_t WS_RG = 0;
constexpr size_t WS_XT0 = WS_RG + (size_t)D * RG_STRIDE * 2;
constexpr size_t WS_XT1 = WS_XT0 + SZ_XT;
constexpr size_t WS_XT2 = WS_XT1 + SZ_XT;
constexpr size_t WS_XT3 = WS_XT2 + SZ_XT;
constexpr size_t WS_ST0 = WS_XT3 + SZ_XT;
constexpr size_t WS_ST2 = WS_ST0 + (size_t)L * 8;
constexpr size_t WS_SF2 = WS_ST2 + (size_t)L * 4 * 8;
constexpr size_t WS_CNT = WS_SF2 + (size_t)L * 8;
constexpr size_t WS_CS = WS_CNT + (size_t)3 * 64 * 128;
constexpr size_t WS_WQ = WS_CS + 4096;
constexpr size_t WS_WO = WS_WQ + (size_t)44 * 20 * 1024;
constexpr size_t WS_Q = WS_WO + (size_t)40 * 20 * 1024;
constexpr size_t WS_KV = WS_Q + SZ_XT;
constexpr size_t WS_O = WS_KV + (size_t)64 * 4096;
constexpr size_t WS_END = WS_O + SZ_XT;

#define STAMP(kid, i) do {} while (0)
DI float bf2f(u16 v) { return __uint_as_float(((unsigned)v) << 16); }
DI float bflo(unsigned w) { return __uint_as_float(w << 16); }
DI float bfhi(unsigned w) { return __uint_as_float(w & 0xffff0000u); }
DI unsigned pk2(float a, float b) {
  f32x2 f = {a, b};
  bf16x2 h = __builtin_convertvector(f, bf16x2);
  return __builtin_bit_cast(unsigned, h);
}
DI float rbf(float a) {
  return __uint_as_float(pk2(a, 0.f) << 16);
}
DI int crow(int reg, int h) { return (reg & 3) + 8 * (reg >> 2) + 4 * h; }
typedef __attribute__((address_space(3))) s16x4 lds_s16x4;
DI s16x4 tr_read(const char* p) { return __builtin_amdgcn_ds_read_tr16_b64_v4i16((lds_s16x4*)p); }

DI void cexpf_(float re, float im, float p, float& er, float& ei) {
  float ar = re * p, ai = im * p;
  float mag = __builtin_amdgcn_exp2f(ar * 1.4426950408889634f);
  float n = __builtin_rintf(ai * 0.15915494309189535f);
  float r = __builtin_fmaf(-n, 6.2831854820251465f, ai);
  r = __builtin_fmaf(-n, -1.7484555314695172e-07f, r);
  float rev = r * 0.15915494309189535f;
  er = mag * __builtin_amdgcn_cosf(rev);
  ei = mag * __builtin_amdgcn_sinf(rev);
}

DI void ln_params(float sum, float m2, float& mb, float& sb) {
  mb = rbf(sum * (1.0f / D));
  float vb = rbf(m2 * (1.0f / D));
  sb = __builtin_amdgcn_rcpf(rbf(__builtin_amdgcn_sqrtf(rbf(vb + 1e-5f))));
}

constexpr int PREP_KGEN = 640, PREP_LN = 128, PREP_WQ = 100, PREP_WO = 100, PREP_WKV = 20, PREP_CS = 11, PREP_KV = 1;
constexpr int PREP_BLOCKS = PREP_KGEN + PREP_LN + PREP_WQ + PREP_WO + PREP_WKV + PREP_CS + PREP_KV;

DI void cexp_rev(float e0, float e1, float f0, float f1, float p, float& er, float& ei) {
  const float mag = __builtin_amdgcn_exp2f(__builtin_fmaf(e1, p, e0));
  const float rev = __builtin_amdgcn_fractf(__builtin_fmaf(f1, p, f0));
  er = mag * __builtin_amdgcn_cosf(rev);
  ei = mag * __builtin_amdgcn_sinf(rev);
}

__global__ __launch_bounds__(256) void prep_kernel(const u16* __restrict__ xu, const u16* __restrict__ lar,
                                                   const u16* __restrict__ aim, const u16* __restrict__ cre,
                                                   const u16* __restrict__ cim, const u16* __restrict__ ldt,
                                                   const u16* __restrict__ wq, const u16* __restrict__ wk,
                                                   const u16* __restrict__ wv, const u16* __restrict__ wo,
                                                   char* __restrict__ ws) {
  __shared__ __attribute__((aligned(16))) char smem[16 * 1296 + 64];
  const int tid = threadIdx.x, lane = tid & 63, w = tid >> 6;
  int bid = blockIdx.x;
  if (bid < PREP_KGEN) {
    const int d = bid;
    f32x4* prm = (f32x4*)smem;
    float* part = (float*)(smem + 2048);
    float* ktab = (float*)(smem + 2048 + 12288);
    if (tid < NS) {
      const float dt = rbf(expf(bf2f(ldt[d])));
      const float are = -rbf(expf(bf2f(lar[d * NS + tid])));
      const float ai = bf2f(aim[d * NS + tid]);
      const float cr = bf2f(cre[d * NS + tid]) * dt, ci = bf2f(cim[d * NS + tid]) * dt;
      const float mx = fmaxf(fabsf(cr), fabsf(ci));
      float lc = -1000.f, ph = 0.f;
      if (mx > 0.f) {
        const float a = cr / mx, b = ci / mx;
        lc = log2f(mx) + 0.5f * log2f(a * a + b * b);
        ph = atan2f(ci, cr) * 0.15915494309189535f;
      }
      f32x4 p = {are * dt * 1.4426950408889634f, ai * dt * 0.15915494309189535f, lc, ph};
      prm[tid] = p;
    }
    __syncthreads();
    const int i = lane & 15, kq = lane >> 4;
    const float A0 = (float)(128 * (i >> 3) + (i & 7));
    const float A1 = (float)(i < 8 ? 256 + 128 * (i >> 2) + 2 * (i & 3) : 512 + 128 * ((i - 8) >> 1) + 4 * (i & 1));
    const float A2 = (float)(i <= 8 ? 1024 + 128 * i : 0);
    const float Bp = (float)(8 * i);
    f32x4 acc0 = {}, acc1 = {}, acc2 = {};
#pragma unroll 2
    for (int t = 0; t < 8; ++t) {
      const f32x4 p = prm[32 * w + 4 * t + kq];
      float br, bi, ar, ai_;
      cexp_rev(0.f, p[0], 0.f, p[1], Bp, br, bi);
      cexp_rev(p[2], p[0], p[3], p[1], A0, ar, ai_);
      acc0 = __builtin_amdgcn_mfma_f32_16x16x4f32(ar, br, acc0, 0, 0, 0);
      acc0 = __builtin_amdgcn_mfma_f32_16x16x4f32(-ai_, bi, acc0, 0, 0, 0);
      cexp_rev(p[2], p[0], p[3], p[1], A1, ar, ai_);
      acc1 = __builtin_amdgcn_mfma_f32_16x16x4f32(ar, br, acc1, 0, 0, 0);
      acc1 = __builtin_amdgcn_mfma_f32_16x16x4f32(-ai_, bi, acc1, 0, 0, 0);
      cexp_rev(p[2], p[0], p[3], p[1], A2, ar, ai_);
      acc2 = __builtin_amdgcn_mfma_f32_16x16x4f32(ar, br, acc2, 0, 0, 0);
      acc2 = __builtin_amdgcn_mfma_f32_16x16x4f32(-ai_, bi, acc2, 0, 0, 0);
    }
#pragma unroll
    for (int r = 0; r < 4; ++r) {
      const int idx = (4 * kq + r) * 16 + i;
      part[(w * 3 + 0) * 256 + idx] = acc0[r];
      part[(w * 3 + 1) * 256 + idx] = acc1[r];
      part[(w * 3 + 2) * 256 + idx] = acc2[r];
    }
    __syncthreads();
    {
      const int r = tid >> 4, b8 = 8 * (tid & 15);
      const int p0 = 128 * (r >> 3) + (r & 7) + b8;
      const int p1 = (r < 8 ? 256 + 128 * (r >> 2) + 2 * (r & 3) : 512 + 128 * ((r - 8) >> 1) + 4 * (r & 1)) + b8;
      const int p2 = 1024 + 128 * r + b8;
      const float k0 = 2.f * (part[0 * 256 + tid] + part[3 * 256 + tid] + part[6 * 256 + tid] + part[9 * 256 + tid]);
      const float k1 = 2.f * (part[1 * 256 + tid] + part[4 * 256 + tid] + part[7 * 256 + tid] + part[10 * 256 + tid]);
      const float k2 = 2.f * (part[2 * 256 + tid] + part[5 * 256 + tid] + part[8 * 256 + tid] + part[11 * 256 + tid]);
      ktab[p0] = k0;
      ktab[r < 8 ? 128 + (p1 >> 1) : 256 + (p1 >> 2)] = k1;
      if (p2 <= 2048) ktab[384 + (p2 >> 3)] = k2;
    }
    __syncthreads();
    float v[8];
#pragma unroll
    for (int e = 0; e < 8; ++e) {
      const int l = 2047 - (8 * tid + e);
      const int lc = l < 1 ? 1 : l;
      const int sh = max(24 - __builtin_clz((unsigned)lc), 0);
      const int add = sh ? ((1 << sh) >> 1) - 1 + ((lc >> sh) & 1) : 0;
      const int idx = l < 1 ? 0 : 128 * sh + ((lc + add) >> sh);
      const float kv = ktab[idx];
      v[e] = l < 0 ? 0.f : kv;
    }
    u16* rgd = (u16*)(ws + WS_RG) + (size_t)d * RG_STRIDE;
    *(u32x4*)(rgd + 8 * tid) = u32x4{pk2(v[0], v[1]), pk2(v[2], v[3]), pk2(v[4], v[5]), pk2(v[6], v[7])};
    if (tid == 0) *(u32x4*)(rgd + 2048) = u32x4{0u, 0u, 0u, 0u};
    return;
  }
  bid -= PREP_KGEN;
  if (bid < PREP_LN) {
    const int l0 = bid * 16;
    const int row = tid >> 4, t16 = tid & 15;
    const u16* src = xu + (size_t)(l0 + row) * D;
    u32x4 v[5];
#pragma unroll
    for (int i = 0; i < 5; ++i) v[i] = *(const u32x4*)(src + (t16 + 16 * i) * 8);
    float s = 0.f;
#pragma unroll
    for (int i = 0; i < 5; ++i) {
#pragma unroll
      for (int j = 0; j < 4; ++j) s += bflo(v[i][j]) + bfhi(v[i][j]);
      *(u32x4*)(smem + row * 1296 + (t16 + 16 * i) * 16) = v[i];
    }
    s += __shfl_xor(s, 1); s += __shfl_xor(s, 2); s += __shfl_xor(s, 4); s += __shfl_xor(s, 8);
    const float mean = s * (1.0f / D);
    float m2 = 0.f;
#pragma unroll
    for (int i = 0; i < 5; ++i)
#pragma unroll
      for (int j = 0; j < 4; ++j) {
        float a = bflo(v[i][j]) - mean, b = bfhi(v[i][j]) - mean;
        m2 += a * a + b * b;
      }
    m2 += __shfl_xor(m2, 1); m2 += __shfl_xor(m2, 2); m2 += __shfl_xor(m2, 4); m2 += __shfl_xor(m2, 8);
    if (t16 == 0) {
      float mb, sb;
      ln_params(s, m2, mb, sb);
      f32x2 o = {mb, sb};
      *(f32x2*)(ws + WS_ST0 + (size_t)(l0 + row) * 8) = o;
    }
    __syncthreads();
    u16* xt = (u16*)(ws + WS_XT0);
    const int g = lane >> 4, q = (lane & 15) >> 2, p = lane & 3, i16 = lane & 15;
#pragma unroll
    for (int it = 0; it < 5; ++it) {
      const int cb = (it * 4 + w) * 2 + (g >> 1);
      const char* base = smem + (8 * (g & 1) + q) * 1296 + cb * 32 + 8 * p;
      s16x4 lo = tr_read(base);
      s16x4 hi = tr_read(base + 4 * 1296);
      u32x2 a = __builtin_bit_cast(u32x2, lo), b = __builtin_bit_cast(u32x2, hi);
      u32x4 o = {a[0], a[1], b[0], b[1]};
      *(u32x4*)(xt + (size_t)(cb * 16 + i16) * LT + l0 + 8 * (g & 1)) = o;
    }
    return;
  }
  bid -= PREP_LN;
  if (bid < PREP_WQ + PREP_WO) {
    const bool isq = bid < PREP_WQ;
    const int t = isq ? bid : bid - PREP_WQ;
    const int ks = t / 5, nb = t % 5;
    const u16* src = (isq ? wq : wo) + (size_t)(32 * ks) * D + 128 * nb;
#pragma unroll
    for (int i = 0; i < 2; ++i) {
      const int c = tid + 256 * i, r = c >> 4, cc = c & 15;
      *(u32x4*)(smem + r * 272 + cc * 16) = *(const u32x4*)(src + (size_t)r * D + cc * 8);
    }
    __syncthreads();
    const int g = lane >> 4, q = (lane & 15) >> 2, p = lane & 3;
#pragma unroll
    for (int i = 0; i < 2; ++i) {
      const int ntl = w * 2 + i;
      const char* base = smem + (8 * g + q) * 272 + ntl * 32 + 8 * p;
      s16x4 lo = tr_read(base);
      s16x4 hi = tr_read(base + 4 * 272);
      u32x2 a = __builtin_bit_cast(u32x2, lo), b = __builtin_bit_cast(u32x2, hi);
      u32x4 o = {a[0], a[1], b[0], b[1]};
      *(u32x4*)(ws + (isq ? WS_WQ : WS_WO) + (size_t)((8 * nb + ntl) * 20 + ks) * 1024 + lane * 16) = o;
    }
    return;
  }
  bid -= PREP_WQ + PREP_WO;
  if (bid < PREP_WKV) {
    const int ks = bid, nt = 40 + w;
    const int n = (lane & 15), k0 = 32 * ks + 8 * (lane >> 4);
    const u16* src = (w < 2) ? wk : wv;
    const int col = 16 * (w & 1) + n;
    const bool valid = col < DH;
    unsigned e[8];
#pragma unroll
    for (int j = 0; j < 8; ++j) e[j] = valid ? (unsigned)src[(size_t)(k0 + j) * DH + col] : 0u;
    u32x4 o = {e[0] | (e[1] << 16), e[2] | (e[3] << 16), e[4] | (e[5] << 16), e[6] | (e[7] << 16)};
    *(u32x4*)(ws + WS_WQ + (size_t)(nt * 20 + ks) * 1024 + lane * 16) = o;
    return;
  }
  bid -= PREP_WKV;
  if (bid < PREP_CS) {
    float* red = (float*)smem;
    if (bid < 10) {
      const int cg = tid & 7, kg = tid >> 3;
      const u16* src = wq + (size_t)(20 * kg) * D + 64 * bid + 8 * cg;
      float sm[8];
#pragma unroll
      for (int e = 0; e < 8; ++e) sm[e] = 0.f;
#pragma unroll
      for (int k = 0; k < 20; ++k) {
        const u32x4 v = *(const u32x4*)(src + (size_t)k * D);
#pragma unroll
        for (int j = 0; j < 4; ++j) { sm[2 * j] += bflo(v[j]); sm[2 * j + 1] += bfhi(v[j]); }
      }
#pragma unroll
      for (int e = 0; e < 8; ++e) red[kg * 64 + 8 * cg + e] = sm[e];
    } else {
      for (int c = tid; c < 32 * 64; c += 256) red[c] = 0.f;
      __syncthreads();
      for (int it = tid; it < 640; it += 256) {
        const int kg = it / 20, cp = it % 20;
        const u16* src = (cp < 10 ? wk : wv) + (size_t)(20 * kg) * DH + 2 * (cp % 10);
        float a = 0.f, b = 0.f;
#pragma unroll
        for (int k = 0; k < 20; ++k) {
          const unsigned v = *(const unsigned*)(src + (size_t)k * DH);
          a += bflo(v); b += bfhi(v);
        }
        const int col = (cp < 10 ? 0 : 32) + 2 * (cp % 10);
        red[kg * 64 + col] = a; red[kg * 64 + col + 1] = b;
      }
    }
    __syncthreads();
    if (tid < 64) {
      float t = 0.f;
#pragma unroll
      for (int kg = 0; kg < 32; ++kg) t += red[kg * 64 + tid];
      ((float*)(ws + WS_CS))[64 * bid + tid] = t;
    }
    return;
  }
  bid -= PREP_CS;
  {
    if (bid == 0) {
      for (int c = tid; c < 3 * 64 * 32; c += 256) ((unsigned*)(ws + WS_CNT))[c] = 0u;
    }
    for (int c = bid * 256 + tid; c < 64 * 4; c += PREP_KV * 256) {
      u32x4 o = {0x3f803f80u, 0x3f803f80u, 0x3f803f80u, 0x3f803f80u};
      *(u32x4*)(ws + WS_KV + (size_t)(c >> 2) * TS + TV + (c & 3) * TVB + DH * 16) = o;
    }
  }
}

constexpr int CS = 4160;
__global__ __launch_bounds__(256) void conv_kernel(const u16* __restrict__ xt, const f32x4* __restrict__ stats,
                                                   const u16* __restrict__ rg, const u16* __restrict__ dskip,
                                                   u16* __restrict__ xout) {
  __shared__ __attribute__((aligned(16))) char smem[8 * CS + 96 * 80];
  char* KC = smem;
  char* U = smem + 8 * CS;
  const int tid = threadIdx.x, lane = tid & 63, w = tid >> 6;
  const int d = blockIdx.x;
  const u16* rgd = rg + (size_t)d * RG_STRIDE;
  const u32x4 xv = *(const u32x4*)(xt + (size_t)d * LT + 8 * tid);
  float mb[8], sb[8];
#pragma unroll
  for (int e = 0; e < 4; ++e) {
    const f32x4 s = stats[4 * tid + e];
    mb[2 * e] = s[0]; sb[2 * e] = s[1]; mb[2 * e + 1] = s[2]; sb[2 * e + 1] = s[3];
  }
  const u32x4 r0 = *(const u32x4*)(rgd + 8 * tid);
  const u32x4 r1 = *(const u32x4*)(rgd + 8 * tid + 8);
  const float dsk = bf2f(dskip[d]);
  {
    unsigned wv[8] = {r0[0], r0[1], r0[2], r0[3], r1[0], r1[1], r1[2], r1[3]};
#pragma unroll
    for (int s = 0; s < 8; ++s) {
      u32x4 o;
#pragma unroll
      for (int k = 0; k < 4; ++k) {
        if (s & 1) o[k] = __builtin_amdgcn_alignbit(wv[(s >> 1) + k + 1], wv[(s >> 1) + k], 16);
        else o[k] = wv[(s >> 1) + k];
      }
      *(u32x4*)(KC + s * CS + tid * 16) = o;
    }
    if (tid < 32) {
      u32x4 z = {0u, 0u, 0u, 0u};
      *(u32x4*)(KC + (tid >> 2) * CS + (256 + (tid & 3)) * 16) = z;
    }
  }
  float xf[8], uf[8];
#pragma unroll
  for (int j = 0; j < 4; ++j) { xf[2 * j] = bflo(xv[j]); xf[2 * j + 1] = bfhi(xv[j]); }
#pragma unroll
  for (int e = 0; e < 8; ++e) uf[e] = rbf(rbf(xf[e] - mb[e]) * sb[e]);
  {
    u32x4 o = {pk2(uf[0], uf[1]), pk2(uf[2], uf[3]), pk2(uf[4], uf[5]), pk2(uf[6], uf[7])};
    *(u32x4*)(U + ((tid >> 2) + 32) * 80 + (tid & 3) * 16) = o;
    if (tid < 128) {
      u32x4 z = {0u, 0u, 0u, 0u};
      *(u32x4*)(U + (tid >> 2) * 80 + (tid & 3) * 16) = z;
    }
  }
  __syncthreads();
  const int b = lane & 31, h = lane >> 5;
  const char* kbase = KC + (7 - (b & 7)) * CS + 16 * (255 - (b >> 3) + h);
  const char* ubase = U + (b + 32) * 80 + 16 * h;
  f32x16 acc0 = {}, acc1 = {};
  const int c0 = w == 0 ? 0 : (w == 1 ? 12 : 24);
  const int nboth = w < 2 ? 12 : (w == 2 ? 8 : 0);
  const int c1 = w == 3 ? 40 : 32, nsing = w < 2 ? 0 : (w == 2 ? 8 : 24);
  if (nboth) {
    const char* kp = kbase - 64 * c0;
    const char* up = ubase - 80 * c0;
    bf16x8 kf0 = *(const bf16x8*)(kp), kf1 = *(const bf16x8*)(kp + 32);
    bf16x8 a0 = *(const bf16x8*)(up), a1 = *(const bf16x8*)(up + 32);
    bf16x8 b0 = *(const bf16x8*)(up + 2560), b1 = *(const bf16x8*)(up + 2560 + 32);
    for (int c = 0; c < nboth; ++c) {
      kp -= 64; up -= 80;
      const bf16x8 nk0 = *(const bf16x8*)(kp), nk1 = *(const bf16x8*)(kp + 32);
      const bf16x8 na0 = *(const bf16x8*)(up), na1 = *(const bf16x8*)(up + 32);
      const bf16x8 nb0 = *(const bf16x8*)(up + 2560), nb1 = *(const bf16x8*)(up + 2560 + 32);
      acc0 = __builtin_amdgcn_mfma_f32_32x32x16_bf16(a0, kf0, acc0, 0, 0, 0);
      acc1 = __builtin_amdgcn_mfma_f32_32x32x16_bf16(b0, kf0, acc1, 0, 0, 0);
      acc0 = __builtin_amdgcn_mfma_f32_32x32x16_bf16(a1, kf1, acc0, 0, 0, 0);
      acc1 = __builtin_amdgcn_mfma_f32_32x32x16_bf16(b1, kf1, acc1, 0, 0, 0);
      kf0 = nk0; kf1 = nk1; a0 = na0; a1 = na1; b0 = nb0; b1 = nb1;
    }
  }
  if (nsing) {
    const char* kp = kbase - 64 * c1;
    const char* up = ubase + 80 * (32 - c1);
    bf16x8 kf0 = *(const bf16x8*)(kp), kf1 = *(const bf16x8*)(kp + 32);
    bf16x8 b0 = *(const bf16x8*)(up), b1 = *(const bf16x8*)(up + 32);
    for (int c = 0; c < nsing; ++c) {
      const bool more = c + 1 < nsing;
      kp -= more ? 64 : 0; up -= more ? 80 : 0;
      const bf16x8 nk0 = *(const bf16x8*)(kp), nk1 = *(const bf16x8*)(kp + 32);
      const bf16x8 nb0 = *(const bf16x8*)(up), nb1 = *(const bf16x8*)(up + 32);
      acc1 = __builtin_amdgcn_mfma_f32_32x32x16_bf16(b0, kf0, acc1, 0, 0, 0);
      acc1 = __builtin_amdgcn_mfma_f32_32x32x16_bf16(b1, kf1, acc1, 0, 0, 0);
      kf0 = nk0; kf1 = nk1; b0 = nb0; b1 = nb1;
    }
  }
  __syncthreads();
  float* part = (float*)smem;
#pragma unroll
  for (int r = 0; r < 16; ++r) {
    const int a = crow(r, h);
    part[w * 2048 + 32 * a + b] = acc0[r];
    part[w * 2048 + 1024 + 32 * a + b] = acc1[r];
  }
  __syncthreads();
  float y[8];
#pragma unroll
  for (int e = 0; e < 8; ++e) y[e] = 0.f;
#pragma unroll
  for (int ww = 0; ww < 4; ++ww) {
    const f32x4 p0 = *(const f32x4*)(part + ww * 2048 + 8 * tid);
    const f32x4 p1 = *(const f32x4*)(part + ww * 2048 + 8 * tid + 4);
#pragma unroll
    for (int e = 0; e < 4; ++e) { y[e] += p0[e]; y[4 + e] += p1[e]; }
  }
  float o[8];
#pragma unroll
  for (int e = 0; e < 8; ++e) o[e] = xf[e] + rbf(rbf(y[e]) + rbf(uf[e] * dsk));
  u32x4 ov = {pk2(o[0], o[1]), pk2(o[2], o[3]), pk2(o[4], o[5]), pk2(o[6], o[7])};
  *(u32x4*)(xout + (size_t)d * LT + 8 * tid) = ov;
}

__global__ __launch_bounds__(512, 2) void qkv_kernel(const u16* __restrict__ xt, const char* __restrict__ wf,
                                                     const float* __restrict__ csum, u16* __restrict__ Q,
                                                     char* __restrict__ kvf) {
  __shared__ __attribute__((aligned(16))) char smem[640 * 128 + 512 + 6 * 5 * 1024];
  f32x2* lnp = (f32x2*)(smem + 640 * 128);
  const int tid = threadIdx.x, lane = tid & 63, w = tid >> 6;
  const int idx = blockIdx.x >> 3;
  const int rt = (idx >> 3) * 8 + (blockIdx.x & 7), ns = idx & 7;
  const int l0 = 64 * rt;
  {
    const int dr = lane >> 3, c = lane & 7;
    const int sw = ((dr >> 1) & 1) | ((w & 1) << 1);
#pragma unroll
    for (int i = 0; i < 10; ++i) {
      const int piece = i * 8 + w;
      const int d = 8 * piece + dr;
      const u16* src = xt + (size_t)d * LT + l0 + 8 * (c ^ (sw << 1));
      __builtin_amdgcn_global_load_lds((const __attribute__((address_space(1))) void*)src,
                                       (__attribute__((address_space(3))) void*)(smem + piece * 1024), 16, 0, 0);
    }
  }
  const int P = w % 3, Hh = w / 3;
  const int ntp = ns * 6 + 2 * P;
  const bool active = (w < 6) && (ntp < 44);
  const char* wbase = wf + (size_t)((active ? ntp : 0) * 20 + 10 * Hh) * 1024 + lane * 16;
  bf16x8 w0[10], w1[10];
  const f32x4 accc = *(const f32x4*)(csum + 16 * ((active ? ntp : 0) + Hh % 2) + 4 * (lane >> 4));
  if (active) {
#pragma unroll
    for (int ks = 0; ks < 10; ++ks) w0[ks] = *(const bf16x8*)(wbase + ks * 1024);
#pragma unroll
    for (int ks = 0; ks < 10; ++ks) w1[ks] = *(const bf16x8*)(wbase + 20 * 1024 + ks * 1024);
  }
  __syncthreads();
  const int g = lane >> 4, q = (lane & 15) >> 2, p = lane & 3;
  const int sw = ((q >> 1) & 1) | ((g & 1) << 1);
  const char* ub = smem + (8 * g + q) * 128 + 8 * p;
  const u32x4 onesu = {0x3f803f80u, 0x3f803f80u, 0x3f803f80u, 0x3f803f80u};
  const bf16x8 ones = __builtin_bit_cast(bf16x8, onesu);
  f32x4 acc[4];
  f32x4* xch = (f32x4*)(smem + 640 * 128 + 512);
#pragma unroll
  for (int a = 0; a < 4; ++a) acc[a] = f32x4{};
  if (active) {
    f32x4 bcc[4];
#pragma unroll
    for (int a = 0; a < 4; ++a) bcc[a] = f32x4{};
    const char* ubh = ub + Hh * 10 * 4096;
    bf16x8 uf[2][8];
    auto rd2 = [&](int kp, bf16x8* dst) {
#pragma unroll
      for (int kk = 0; kk < 2; ++kk)
#pragma unroll
        for (int lt = 0; lt < 4; ++lt) {
          const char* pp = ubh + (2 * kp + kk) * 4096 + ((lt ^ sw) << 5);
          s16x4 a0 = tr_read(pp), a1 = tr_read(pp + 512);
          dst[kk * 4 + lt] = __builtin_bit_cast(bf16x8, __builtin_shufflevector(a0, a1, 0, 1, 2, 3, 4, 5, 6, 7));
        }
    };
    rd2(0, uf[0]);
#pragma unroll
    for (int kp = 0; kp < 5; ++kp) {
      __builtin_amdgcn_sched_barrier(0);
      if (kp + 1 < 5) rd2(kp + 1, uf[(kp + 1) & 1]);
      __builtin_amdgcn_sched_barrier(0);
#pragma unroll
      for (int kk = 0; kk < 2; ++kk) {
        const int ks = 2 * kp + kk;
#pragma unroll
        for (int lt = 0; lt < 4; ++lt) {
          acc[lt] = __builtin_amdgcn_mfma_f32_16x16x32_bf16(w0[ks], uf[kp & 1][kk * 4 + lt], acc[lt], 0, 0, 0);
          bcc[lt] = __builtin_amdgcn_mfma_f32_16x16x32_bf16(w1[ks], uf[kp & 1][kk * 4 + lt], bcc[lt], 0, 0, 0);
        }
      }
    }
    if (Hh) {
#pragma unroll
      for (int a = 0; a < 4; ++a) { xch[(w * 5 + a) * 64 + lane] = acc[a]; acc[a] = bcc[a]; }
    } else {
#pragma unroll
      for (int a = 0; a < 4; ++a) xch[(w * 5 + a) * 64 + lane] = bcc[a];
    }
  } else if (w >= 6) {
    bf16x8 uf[2][8];
    auto rd4 = [&](int kp, bf16x8* dst) {
#pragma unroll
      for (int kk = 0; kk < 4; ++kk)
#pragma unroll
        for (int j = 0; j < 2; ++j) {
          const char* pp = ub + (4 * kp + kk) * 4096 + (((2 * (w - 6) + j) ^ sw) << 5);
          s16x4 a0 = tr_read(pp), a1 = tr_read(pp + 512);
          dst[kk * 2 + j] = __builtin_bit_cast(bf16x8, __builtin_shufflevector(a0, a1, 0, 1, 2, 3, 4, 5, 6, 7));
        }
    };
    rd4(0, uf[0]);
#pragma unroll
    for (int kp = 0; kp < 5; ++kp) {
      __builtin_amdgcn_sched_barrier(0);
      if (kp + 1 < 5) rd4(kp + 1, uf[(kp + 1) & 1]);
      __builtin_amdgcn_sched_barrier(0);
#pragma unroll
      for (int kk = 0; kk < 4; ++kk)
#pragma unroll
        for (int j = 0; j < 2; ++j) {
          const bf16x8 u0 = uf[kp & 1][kk * 2 + j];
          acc[j] = __builtin_amdgcn_mfma_f32_16x16x32_bf16(ones, u0, acc[j], 0, 0, 0);
          acc[2 + j] = __builtin_amdgcn_mfma_f32_16x16x32_bf16(u0, u0, acc[2 + j], 0, 0, 0);
        }
    }
    const int li = lane & 15;
    if (g == (li >> 2)) {
#pragma unroll
      for (int j = 0; j < 2; ++j) {
        const float sx = acc[j][0];
        const f32x4 gm = acc[2 + j];
        const int r = li & 3;
        const float sxx = r == 0 ? gm[0] : (r == 1 ? gm[1] : (r == 2 ? gm[2] : gm[3]));
        float mb, sb;
        ln_params(sx, fmaxf(sxx - sx * sx * (1.0f / D), 0.f), mb, sb);
        f32x2 o = {mb, sb};
        lnp[16 * (2 * (w - 6) + j) + li] = o;
      }
    }
  }
  __syncthreads();
  if (!active) return;
  const int nt = ntp + Hh;
  {
    const int pw = Hh ? w - 3 : w + 3;
#pragma unroll
    for (int a = 0; a < 4; ++a) acc[a] += xch[(pw * 5 + a) * 64 + lane];
  }
#pragma unroll
  for (int lt = 0; lt < 4; ++lt) {
    const f32x2 pr = lnp[16 * lt + (lane & 15)];
#pragma unroll
    for (int e = 0; e < 4; ++e) acc[lt][e] = (acc[lt][e] - pr[0] * accc[e]) * pr[1];
  }
  const float qscale = 0.22360679774997896f * 1.4426950408889634f;
#pragma unroll
  for (int lt = 0; lt < 4; ++lt) {
    const int l = l0 + 16 * lt + (lane & 15);
    const f32x4 r = acc[lt];
    if (nt < 40) {
      u32x2 o = {pk2(r[0] * qscale, r[1] * qscale), pk2(r[2] * qscale, r[3] * qscale)};
      *(u32x2*)(Q + (size_t)l * D + 16 * nt + 4 * g) = o;
    } else if (nt < 42) {
      const int r32 = l & 31;
      u32x2 o = {pk2(r[0], r[1]), pk2(r[2], r[3])};
      if (nt == 40) *(u32x2*)(kvf + (size_t)(l >> 5) * TS + (r32 + 32 * (g >> 1)) * 16 + 8 * (g & 1)) = o;
      else if (g == 0) *(u32x2*)(kvf + (size_t)(l >> 5) * TS + TK1 + r32 * 8) = o;
    } else {
      const int r32 = l & 31;
      const int s_ = r32 >> 4, jh = (r32 >> 3) & 1, hh = (r32 >> 2) & 1, jl = r32 & 3;
      const int j = 4 * jh + jl;
#pragma unroll
      for (int e = 0; e < 4; ++e) {
        const int dv = 16 * (nt - 42) + 4 * g + e;
        if (dv < DH) {
          u16 bv = (u16)(pk2(r[e], 0.f) & 0xffffu);
          *(u16*)(kvf + (size_t)(l >> 5) * TS + TV + (2 * s_ + hh) * TVB + dv * 16 + 2 * j) = bv;
        }
      }
    }
  }
}

constexpr int ACH = 11;
constexpr int CBUF = 29 * 1024;
constexpr int WLK = 10;
struct KVFrag { bf16x8 k0, k1, v0, v1; };
struct KVOff { int k0, k1, v0; };
DI KVOff kv_offsets(int lane) {
  const int r = lane & 31, h = lane >> 5;
  KVOff o;
  o.k0 = lane * 16;
  o.k1 = TK1 + r * 8;
  o.v0 = TV + h * TVB + (r < DH ? r : DH) * 16;
  return o;
}
DI void kv_load_k(const char* tb, const KVOff& f, bf16x8& k0, bf16x8& k1) {
  k0 = *(const bf16x8*)(tb + f.k0);
  const u32x2 t = *(const u32x2*)(tb + f.k1);
  k1 = __builtin_bit_cast(bf16x8, u32x4{t[0], t[1], 0u, 0u});
}
DI void kv_load_v(const char* tb, const KVOff& f, bf16x8& v0, bf16x8& v1) {
  v0 = *(const bf16x8*)(tb + f.v0);
  v1 = *(const bf16x8*)(tb + f.v0 + 2 * TVB);
}
DI void attn_safe_tile(const char* tb, const KVOff& f, const bf16x8& qf0, const bf16x8& qf1, f32x16& o, f32x16& negm,
                       bool first) {
  bf16x8 k0, k1, v0, v1;
  kv_load_k(tb, f, k0, k1);
  kv_load_v(tb, f, v0, v1);
  f32x16 s = __builtin_amdgcn_mfma_f32_32x32x16_bf16(k0, qf0, negm, 0, 0, 0);
  s = __builtin_amdgcn_mfma_f32_32x32x16_bf16(k1, qf1, s, 0, 0, 0);
  float tm = s[0];
#pragma unroll
  for (int r = 1; r < 16; ++r) tm = fmaxf(tm, s[r]);
  tm = fmaxf(tm, __shfl_xor(tm, 32));
  const float delta = first ? tm : fmaxf(tm, 0.f);
  const float sc = first ? 0.f : __builtin_amdgcn_exp2f(-delta);
#pragma unroll
  for (int r = 0; r < 16; ++r) { o[r] *= sc; negm[r] -= delta; }
  u32x4 pa, pb;
#pragma unroll
  for (int r = 0; r < 4; ++r) {
    pa[r] = pk2(__builtin_amdgcn_exp2f(s[2 * r] - delta), __builtin_amdgcn_exp2f(s[2 * r + 1] - delta));
    pb[r] = pk2(__builtin_amdgcn_exp2f(s[8 + 2 * r] - delta), __builtin_amdgcn_exp2f(s[8 + 2 * r + 1] - delta));
  }
  o = __builtin_amdgcn_mfma_f32_32x32x16_bf16(v0, __builtin_bit_cast(bf16x8, pa), o, 0, 0, 0);
  o = __builtin_amdgcn_mfma_f32_32x32x16_bf16(v1, __builtin_bit_cast(bf16x8, pb), o, 0, 0, 0);
}
#define SBAR() __builtin_amdgcn_sched_barrier(0)
#define EXP4(P, I, S, B)                                                                   \
  P[I] = pk2(__builtin_amdgcn_exp2f(S[B]), __builtin_amdgcn_exp2f(S[B + 1]));              \
  P[I + 1] = pk2(__builtin_amdgcn_exp2f(S[B + 2]), __builtin_amdgcn_exp2f(S[B + 3]));
struct AttnState {
  f32x16 o, s_cur;
  bf16x8 kA0, kA1;
  bf16x8 vA0, vA1;
  bf16x8 vB0, vB1;
  bf16x8 v1p;
  u32x4 pbp;
};
template <int N, bool FIRST, bool LAST, typename F>
DI void attn_chunk(AttnState& st, const char* cur, const char* nxt, const KVOff& f, const bf16x8& qf0,
                   const bf16x8& qf1, const f32x16& negm, F sync) {
  f32x16 s_nxt;
  bf16x8 kB0, kB1, vC0, vC1;
  u32x4 pa, pb;
#pragma unroll
  for (int i = 0; i < N; ++i) {
    const bool has1 = !(LAST && i + 1 >= N);
    const bool has2 = !(LAST && i + 2 >= N);
    SBAR();
    if (!LAST && i == N - 2) {
      asm volatile("" : "+v"(st.o), "+v"(st.pbp), "+v"(st.s_cur));
      sync();
    }
    if (has1) s_nxt = __builtin_amdgcn_mfma_f32_32x32x16_bf16(st.kA0, qf0, negm, 0, 0, 0);
    if (has2) {
      const char* tp = (i + 2 < N) ? cur + (i + 2) * TS : nxt + (i + 2 - N) * TS;
      kv_load_k(tp, f, kB0, kB1);
      kv_load_v(tp, f, vC0, vC1);
    }
    EXP4(pa, 0, st.s_cur, 0);
    if (!(FIRST && i == 0)) st.o = __builtin_amdgcn_mfma_f32_32x32x16_bf16(st.v1p, __builtin_bit_cast(bf16x8, st.pbp), st.o, 0, 0, 0);
    EXP4(pa, 2, st.s_cur, 4);
    if (has1) s_nxt = __builtin_amdgcn_mfma_f32_32x32x16_bf16(st.kA1, qf1, s_nxt, 0, 0, 0);
    EXP4(pb, 0, st.s_cur, 8);
    st.o = __builtin_amdgcn_mfma_f32_32x32x16_bf16(st.vA0, __builtin_bit_cast(bf16x8, pa), st.o, 0, 0, 0);
    EXP4(pb, 2, st.s_cur, 12);
    st.pbp = pb; st.v1p = st.vA1;
    st.s_cur = s_nxt; st.kA0 = kB0; st.kA1 = kB1;
    st.vA0 = st.vB0; st.vA1 = st.vB1; st.vB0 = vC0; st.vB1 = vC1;
  }
  if (LAST) st.o = __builtin_amdgcn_mfma_f32_32x32x16_bf16(st.v1p, __builtin_bit_cast(bf16x8, st.pbp), st.o, 0, 0, 0);
}

typedef __attribute__((address_space(1))) unsigned gu32;
typedef __attribute__((address_space(1))) unsigned long long gu64;
#define RLX_AGENT __ATOMIC_RELAXED, __HIP_MEMORY_SCOPE_AGENT

template <bool FINAL>
__global__ __launch_bounds__(512, 2) void attn_kernel(const u16* __restrict__ Q, const char* __restrict__ kvf,
                                                      u16* O, const char* __restrict__ wf,
                                                      const u16* __restrict__ xres, u16* __restrict__ xout,
                                                      f32x2* pstats, f32x2* __restrict__ fstats, unsigned* cnt) {
  __shared__ __attribute__((aligned(16))) char smem[2 * CBUF + 10 * WLK * 1024];
  const int tid = threadIdx.x, lane = tid & 63, w = tid >> 6;
  const int hq = lane & 31, h = lane >> 5;
  const int idx = blockIdx.x >> 3;
  const int rt = (idx >> 2) * 8 + (blockIdx.x & 7), ns = idx & 3;
  const int row = 32 * rt + 8 * ns + w;
  auto stage = [&](int chunk, int buf) {
    const int np = chunk < 5 ? 29 : 24;
#pragma unroll
    for (int i = 0; i < 4; ++i) {
      const int pc = min(w + 8 * i, np - 1);
      const char* src = kvf + (size_t)chunk * ACH * TS + pc * 1024 + lane * 16;
      __builtin_amdgcn_global_load_lds((const __attribute__((address_space(1))) void*)src,
                                       (__attribute__((address_space(3))) void*)(smem + buf * CBUF + pc * 1024), 16, 0, 0);
    }
  };
  stage(0, 0);
  bf16x8 qf0, qf1;
  {
    const u16* qp = Q + (size_t)row * D + hq * DH;
    u32x2 a = *(const u32x2*)(qp + 8 * h), b = *(const u32x2*)(qp + 8 * h + 4);
    u32x4 t = {a[0], a[1], b[0], b[1]};
    qf0 = __builtin_bit_cast(bf16x8, t);
    u32x2 c = *(const u32x2*)(qp + 16);
    u32x4 t1 = {h ? 0u : c[0], h ? 0u : c[1], 0u, 0u};
    qf1 = __builtin_bit_cast(bf16x8, t1);
  }
  stage(1, 1);
  f32x16 negm = {};
  const int l0 = 32 * rt;
  const int g = lane >> 4, i16 = lane & 15;
  const int nt0 = ns * 10 + 2 * (w < 5 ? w : 0);
  bf16x8 w0[20 - WLK], w1[20 - WLK];
  u32x2 xr[2][2];
  u16 xs[2][2][4];
  asm volatile("s_waitcnt vmcnt(4)" ::: "memory");
  __builtin_amdgcn_s_barrier();
  asm volatile("" ::: "memory");
  const char* buf0 = smem;
  const char* buf1 = smem + CBUF;
  const KVOff kvo = kv_offsets(lane);
  AttnState st;
  if (__builtin_amdgcn_readfirstlane(threadIdx.x) >= 256) __builtin_amdgcn_s_setprio(1);
  {
    bf16x8 k0, k1;
    kv_load_k(buf0, kvo, k0, k1);
    kv_load_v(buf0, kvo, st.vA0, st.vA1);
    kv_load_k(buf0 + TS, kvo, st.kA0, st.kA1);
    kv_load_v(buf0 + TS, kvo, st.vB0, st.vB1);
    f32x16 s = __builtin_amdgcn_mfma_f32_32x32x16_bf16(k0, qf0, negm, 0, 0, 0);
    s = __builtin_amdgcn_mfma_f32_32x32x16_bf16(k1, qf1, s, 0, 0, 0);
    float tm = s[0];
#pragma unroll
    for (int r = 1; r < 16; ++r) tm = fmaxf(tm, s[r]);
    tm = fmaxf(tm, __shfl_xor(tm, 32));
#pragma unroll
    for (int r = 0; r < 16; ++r) { st.s_cur[r] = s[r] - tm; negm[r] = -tm; st.o[r] = 0.f; }
    st.v1p = st.vA1; st.pbp = u32x4{0u, 0u, 0u, 0u};
  }
  const char* wbase = wf + (size_t)(nt0 * 20 + WLK) * 1024 + lane * 16;
  auto sync_stage = [&](int ch) {
    asm volatile("s_waitcnt vmcnt(0) lgkmcnt(0)" ::: "memory");
    __builtin_amdgcn_s_barrier();
    asm volatile("" ::: "memory");
    if (ch + 2 < 6) stage(ch + 2, ch & 1);
    if (ch < 4) {
      for (int f = ch * 25 + w; f < (ch + 1) * 25; f += 8) {
        const int ntl = f / WLK, ks = f % WLK;
        const char* src = wf + (size_t)((ns * 10 + ntl) * 20 + ks) * 1024 + lane * 16;
        __builtin_amdgcn_global_load_lds((const __attribute__((address_space(1))) void*)src,
                                         (__attribute__((address_space(3))) void*)(smem + 2 * CBUF + f * 1024), 16, 0, 0);
      }
    }
  };
  attn_chunk<ACH, true, false>(st, buf0, buf1, kvo, qf0, qf1, negm, [&]() { sync_stage(0); });
  for (int ch = 1; ch < 3; ++ch) {
    const char* cur = (ch & 1) ? buf1 : buf0;
    const char* nxt = (ch & 1) ? buf0 : buf1;
    attn_chunk<ACH, false, false>(st, cur, nxt, kvo, qf0, qf1, negm, [&]() { sync_stage(ch); });
  }
  attn_chunk<ACH, false, false>(st, buf1, buf0, kvo, qf0, qf1, negm, [&]() {
    sync_stage(3);
    if (w < 5) {
#pragma unroll
      for (int ks = 0; ks < 20 - WLK; ++ks) w0[ks] = *(const bf16x8*)(wbase + ks * 1024);
    }
  });
  attn_chunk<ACH, false, false>(st, buf0, buf1, kvo, qf0, qf1, negm, [&]() {
    sync_stage(4);
    if (w < 5) {
#pragma unroll
      for (int ks = 0; ks < 20 - WLK; ++ks) w1[ks] = *(const bf16x8*)(wbase + 20 * 1024 + ks * 1024);
#pragma unroll
      for (int a = 0; a < 2; ++a)
#pragma unroll
        for (int lt = 0; lt < 2; ++lt) {
          if (FINAL) {
#pragma unroll
            for (int e = 0; e < 4; ++e) xs[a][lt][e] = xres[(size_t)(16 * (nt0 + a) + 4 * g + e) * LT + l0 + 16 * lt + i16];
          } else {
            xr[a][lt] = *(const u32x2*)(xres + (size_t)(16 * (nt0 + a) + i16) * LT + l0 + 16 * lt + 4 * g);
          }
        }
    }
  });
  attn_chunk<9, false, true>(st, buf1, buf0, kvo, qf0, qf1, negm, [&]() {});
  __builtin_amdgcn_s_setprio(0);
  f32x16 o = st.o;
  {
    float amax = 0.f;
#pragma unroll
    for (int r = 0; r < 12; ++r) amax = fmaxf(amax, fabsf(o[r]));
    if (__builtin_expect(__any(!(amax < 1.0e30f)), 0)) {
      o = f32x16{};
      negm = f32x16{};
      for (int t = 0; t < 64; ++t) attn_safe_tile(kvf + (size_t)t * TS, kvo, qf0, qf1, o, negm, t == 0);
    }
  }
  __syncthreads();
  {
    float lsum = o[8];
    lsum = __shfl(lsum, hq + 32);
    const float inv = 1.0f / lsum;
#pragma unroll
    for (int g = 0; g < 3; ++g) {
      const int dv = 8 * g + 4 * h;
      if (dv < DH) {
        const int k = hq * DH + dv;
        u32x2 ov = {pk2(o[4 * g] * inv, o[4 * g + 1] * inv), pk2(o[4 * g + 2] * inv, o[4 * g + 3] * inv)};
        *(u32x2*)(smem + ((k >> 3) * 8 + w) * 16 + (k & 7) * 2) = ov;
      }
    }
    __syncthreads();
    const __amdgpu_buffer_rsrc_t rs = __builtin_amdgcn_make_buffer_rsrc((void*)O, 0, L * D * 2, 0x00020000);
    const unsigned obase = (unsigned)((row >> 4) * 20480 + (8 * (ns & 1)) * 16);
#pragma unroll
    for (int i = 0; i < 2; ++i) {
      const int c = tid + 512 * i;
      if (c < 640) {
        const u32x4 t = *(const u32x4*)(smem + c * 16);
        __builtin_amdgcn_raw_buffer_store_b128(t, rs, obase + (unsigned)((c >> 3) * 256 + (c & 7) * 16), 0, 16);
      }
    }
  }
  asm volatile("" ::: "memory");
  asm volatile("s_waitcnt vmcnt(0)" ::: "memory");
  __builtin_amdgcn_s_barrier();
  gu32* c0 = (gu32*)cnt + rt * 32;
  if (tid == 448) {
    __hip_atomic_fetch_add(c0, 1u, RLX_AGENT);
    for (unsigned spins = 0; spins < (1u << 22); ++spins) {
      if (__hip_atomic_load(c0, RLX_AGENT) >= 4u) break;
      __builtin_amdgcn_s_sleep(1);
    }
  }
  asm volatile("" ::: "memory");
  __builtin_amdgcn_s_barrier();
  asm volatile("" ::: "memory");
  {
    const __amdgpu_buffer_rsrc_t rs = __builtin_amdgcn_make_buffer_rsrc((void*)O, 0, L * D * 2, 0x00020000);
    u32x4 t[5];
#pragma unroll
    for (int i = 0; i < 5; ++i)
      t[i] = __builtin_amdgcn_raw_buffer_load_b128(rs, (unsigned)(rt * 40960 + (w * 5 + i) * 1024 + lane * 16), 0, 16);
#pragma unroll
    for (int i = 0; i < 5; ++i) *(u32x4*)(smem + (w * 5 + i) * 1024 + lane * 16) = t[i];
  }
  __syncthreads();
  f32x4 acc[2][2];
  acc[0][0] = f32x4{}; acc[0][1] = f32x4{}; acc[1][0] = f32x4{}; acc[1][1] = f32x4{};
  float* stt = (float*)(smem + 40960);
  if (w < 5) {
    const char* obase = smem + lane * 16;
    const char* wl = smem + 2 * CBUF + (2 * w) * WLK * 1024 + lane * 16;
    bf16x8 fo[2][8], fw[2][8];
    auto rd4 = [&](int kp, bf16x8* od, bf16x8* wd) {
#pragma unroll
      for (int kk = 0; kk < 4; ++kk) {
        const int ks = 4 * kp + kk;
        od[2 * kk] = *(const bf16x8*)(obase + ks * 1024);
        od[2 * kk + 1] = *(const bf16x8*)(obase + 20 * 1024 + ks * 1024);
        wd[2 * kk] = ks < WLK ? *(const bf16x8*)(wl + ks * 1024) : w0[ks < WLK ? 0 : ks - WLK];
        wd[2 * kk + 1] = ks < WLK ? *(const bf16x8*)(wl + (WLK + ks) * 1024) : w1[ks < WLK ? 0 : ks - WLK];
      }
    };
    rd4(0, fo[0], fw[0]);
#pragma unroll
    for (int kp = 0; kp < 5; ++kp) {
      __builtin_amdgcn_sched_barrier(0);
      if (kp + 1 < 5) rd4(kp + 1, fo[(kp + 1) & 1], fw[(kp + 1) & 1]);
      __builtin_amdgcn_sched_barrier(0);
#pragma unroll
      for (int kk = 0; kk < 4; ++kk) {
        const bf16x8 o0 = fo[kp & 1][2 * kk], o1 = fo[kp & 1][2 * kk + 1];
        const bf16x8 wa = fw[kp & 1][2 * kk], wb = fw[kp & 1][2 * kk + 1];
        if (FINAL) {
          acc[0][0] = __builtin_amdgcn_mfma_f32_16x16x32_bf16(wa, o0, acc[0][0], 0, 0, 0);
          acc[0][1] = __builtin_amdgcn_mfma_f32_16x16x32_bf16(wa, o1, acc[0][1], 0, 0, 0);
          acc[1][0] = __builtin_amdgcn_mfma_f32_16x16x32_bf16(wb, o0, acc[1][0], 0, 0, 0);
          acc[1][1] = __builtin_amdgcn_mfma_f32_16x16x32_bf16(wb, o1, acc[1][1], 0, 0, 0);
        } else {
          acc[0][0] = __builtin_amdgcn_mfma_f32_16x16x32_bf16(o0, wa, acc[0][0], 0, 0, 0);
          acc[0][1] = __builtin_amdgcn_mfma_f32_16x16x32_bf16(o1, wa, acc[0][1], 0, 0, 0);
          acc[1][0] = __builtin_amdgcn_mfma_f32_16x16x32_bf16(o0, wb, acc[1][0], 0, 0, 0);
          acc[1][1] = __builtin_amdgcn_mfma_f32_16x16x32_bf16(o1, wb, acc[1][1], 0, 0, 0);
        }
      }
    }
    if (FINAL) {
#pragma unroll
      for (int a = 0; a < 2; ++a)
#pragma unroll
        for (int lt = 0; lt < 2; ++lt) {
          const int l = l0 + 16 * lt + i16;
          const int n = 16 * (nt0 + a) + 4 * g;
          float r[4];
#pragma unroll
          for (int e = 0; e < 4; ++e) r[e] = bf2f(xs[a][lt][e]) + rbf(acc[a][lt][e]);
          u32x2 ov = {pk2(r[0], r[1]), pk2(r[2], r[3])};
          *(u32x2*)(xout + (size_t)l * D + n) = ov;
        }
    } else {
#pragma unroll
      for (int a = 0; a < 2; ++a)
#pragma unroll
        for (int lt = 0; lt < 2; ++lt) {
          const int n = 16 * (nt0 + a) + i16;
          const int l = l0 + 16 * lt + 4 * g;
          float r[4];
          r[0] = rbf(bflo(xr[a][lt][0]) + rbf(acc[a][lt][0]));
          r[1] = rbf(bfhi(xr[a][lt][0]) + rbf(acc[a][lt][1]));
          r[2] = rbf(bflo(xr[a][lt][1]) + rbf(acc[a][lt][2]));
          r[3] = rbf(bfhi(xr[a][lt][1]) + rbf(acc[a][lt][3]));
          u32x2 ov = {pk2(r[0], r[1]), pk2(r[2], r[3])};
          *(u32x2*)(xout + (size_t)n * LT + l) = ov;
          const int nl = 16 * (2 * w + a) + i16;
#pragma unroll
          for (int e = 0; e < 4; ++e) stt[(16 * lt + 4 * g + e) * 164 + nl] = r[e];
        }
    }
  }
  if (!FINAL) {
    __syncthreads();
    unsigned* flag = (unsigned*)(smem + 40960 + 32 * 164 * 4);
    if (tid < 256) {
      const int l = tid >> 3, p8 = tid & 7;
      float v[20];
      float s = 0.f;
#pragma unroll
      for (int e = 0; e < 20; ++e) { v[e] = stt[l * 164 + p8 + 8 * e]; s += v[e]; }
      s += __shfl_xor(s, 1); s += __shfl_xor(s, 2); s += __shfl_xor(s, 4);
      const float mean = s * (1.0f / 160.f);
      float m2 = 0.f;
#pragma unroll
      for (int e = 0; e < 20; ++e) { float a = v[e] - mean; m2 += a * a; }
      m2 += __shfl_xor(m2, 1); m2 += __shfl_xor(m2, 2); m2 += __shfl_xor(m2, 4);
      if (p8 == 0) {
        const unsigned long long pv = (unsigned long long)__float_as_uint(s) | ((unsigned long long)__float_as_uint(m2) << 32);
        __hip_atomic_store((gu64*)(pstats + (size_t)(l0 + l) * 4 + ns), pv, RLX_AGENT);
      }
      asm volatile("s_waitcnt vmcnt(0)" ::: "memory");
    }
    __syncthreads();
    if (tid == 0) *flag = __hip_atomic_fetch_add((gu32*)cnt + (64 + rt) * 32, 1u, RLX_AGENT);
    __syncthreads();
    if (*flag == 3u && tid < 32) {
      float ps[4], pm[4];
      float sum = 0.f;
#pragma unroll
      for (int p = 0; p < 4; ++p) {
        const unsigned long long pv = __hip_atomic_load((gu64*)(pstats + (size_t)(l0 + tid) * 4 + p), RLX_AGENT);
        ps[p] = __uint_as_float((unsigned)pv); pm[p] = __uint_as_float((unsigned)(pv >> 32));
        sum += ps[p];
      }
      const float mean = sum * (1.0f / D);
      float m2 = 0.f;
#pragma unroll
      for (int p = 0; p < 4; ++p) {
        const float dm = ps[p] * (4.0f / D) - mean;
        m2 += pm[p] + dm * dm * (D / 4.0f);
      }
      float mb, sb;
      ln_params(sum, m2, mb, sb);
      f32x2 ov = {mb, sb};
      fstats[l0 + tid] = ov;
    }
  }
}

extern "C" void kernel_launch(void* const* d_in, const int* in_sizes, int n_in, void* d_out, int out_size,
                              void* d_ws, size_t ws_size, hipStream_t stream) {
  const u16* xu = (const u16*)d_in[0];
  const u16* lar = (const u16*)d_in[1];
  const u16* aim = (const u16*)d_in[2];
  const u16* cre = (const u16*)d_in[3];
  const u16* cim = (const u16*)d_in[4];
  const u16* ldt = (const u16*)d_in[5];
  const u16* dsk = (const u16*)d_in[6];
  const u16* wq = (const u16*)d_in[8];
  const u16* wk = (const u16*)d_in[9];
  const u16* wv = (const u16*)d_in[10];
  const u16* wo = (const u16*)d_in[11];
  char* ws = (char*)d_ws;
  u16* out = (u16*)d_out;
  u16* rg = (u16*)(ws + WS_RG);
  u16* xt0 = (u16*)(ws + WS_XT0);
  u16* xt1 = (u16*)(ws + WS_XT1);
  u16* xt2 = (u16*)(ws + WS_XT2);
  u16* xt3 = (u16*)(ws + WS_XT3);
  f32x2* st0 = (f32x2*)(ws + WS_ST0);
  f32x2* st2 = (f32x2*)(ws + WS_ST2);
  u16* Q = (u16*)(ws + WS_Q);
  u16* O = (u16*)(ws + WS_O);
  char* kvf = ws + WS_KV;

  f32x2* sf2 = (f32x2*)(ws + WS_SF2);
  unsigned* cnt = (unsigned*)(ws + WS_CNT);
  prep_kernel<<<PREP_BLOCKS, 256, 0, stream>>>(xu, lar, aim, cre, cim, ldt, wq, wk, wv, wo, ws);
  conv_kernel<<<D, 256, 0, stream>>>(xt0, (const f32x4*)st0, rg, dsk, xt1);
  qkv_kernel<<<256, 512, 0, stream>>>(xt1, ws + WS_WQ, (const float*)(ws + WS_CS), Q, kvf);
  attn_kernel<false><<<256, 512, 0, stream>>>(Q, kvf, O, ws + WS_WO, xt1, xt2, st2, sf2, cnt);
  conv_kernel<<<D, 256, 0, stream>>>(xt2, (const f32x4*)sf2, rg, dsk, xt3);
  qkv_kernel<<<256, 512, 0, stream>>>(xt3, ws + WS_WQ, (const float*)(ws + WS_CS), Q, kvf);
  attn_kernel<true><<<256, 512, 0, stream>>>(Q, kvf, O, ws + WS_WO, xt3, out, st2, sf2, cnt + 128 * 32);
}
```

```cpp
#include <hip/hip_runtime.h>
#include <hip/hip_bf16.h>
#include <stdint.h>

typedef __attribute__((ext_vector_type(8))) __bf16 bf16x8;
typedef __attribute__((ext_vector_type(2))) __bf16 bf16x2;
typedef __attribute__((ext_vector_type(4))) short s16x4;
typedef __attribute__((ext_vector_type(16))) float f32x16;
typedef __attribute__((ext_vector_type(4))) float f32x4;
typedef __attribute__((ext_vector_type(2))) float f32x2;
typedef __attribute__((ext_vector_type(4))) unsigned u32x4;
typedef __attribute__((ext_vector_type(2))) unsigned u32x2;
typedef unsigned short u16;
#define DI __device__ __forceinline__

constexpr int L = 2048, D = 640, NS = 128, H = 32, DH = 20;
constexpr int TS = 2624, TK1 = 1024, TV = 1280, TVB = 336;
constexpr int RG_STRIDE = 2112;

constexpr int LT = 2112;
constexpr size_t SZ_XT = (size_t)D * LT * 2;
constexpr size_t WS_RG = 0;
constexpr size_t WS_XT0 = WS_RG + (size_t)D * RG_STRIDE * 2;
constexpr size_t WS_XT1 = WS_XT0 + SZ_XT;
constexpr size_t WS_XT2 = WS_XT1 + SZ_XT;
constexpr size_t WS_XT3 = WS_XT2 + SZ_XT;
constexpr size_t WS_ST0 = WS_XT3 + SZ_XT;
constexpr size_t WS_ST2 = WS_ST0 + (size_t)L * 8;
constexpr size_t WS_SF2 = WS_ST2 + (size_t)L * 4 * 8;
constexpr size_t WS_CNT = WS_SF2 + (size_t)L * 8;
constexpr size_t WS_CS = WS_CNT + (size_t)3 * 64 * 128;
constexpr size_t WS_WQ = WS_CS + 4096;
constexpr size_t WS_WO = WS_WQ + (size_t)44 * 20 * 1024;
constexpr size_t WS_Q = WS_WO + (size_t)40 * 20 * 1024;
constexpr size_t WS_KV = WS_Q + SZ_XT;
constexpr size_t WS_O = WS_KV + (size_t)64 * 4096;
constexpr size_t WS_END = WS_O + SZ_XT;

DI float bf2f(u16 v) { return __uint_as_float(((unsigned)v) << 16); }
DI float bflo(unsigned w) { return __uint_as_float(w << 16); }
DI float bfhi(unsigned w) { return __uint_as_float(w & 0xffff0000u); }
DI unsigned pk2(float a, float b) {
  f32x2 f = {a, b};
  bf16x2 h = __builtin_convertvector(f, bf16x2);
  return __builtin_bit_cast(unsigned, h);
}
DI float rbf(float a) {
  return __uint_as_float(pk2(a, 0.f) << 16);
}
DI int crow(int reg, int h) { return (reg & 3) + 8 * (reg >> 2) + 4 * h; }
typedef __attribute__((address_space(3))) s16x4 lds_s16x4;
DI s16x4 tr_read(const char* p) { return __builtin_amdgcn_ds_read_tr16_b64_v4i16((lds_s16x4*)p); }

DI void ln_params(float sum, float m2, float& mb, float& sb) {
  mb = rbf(sum * (1.0f / D));
  float vb = rbf(m2 * (1.0f / D));
  sb = __builtin_amdgcn_rcpf(rbf(__builtin_amdgcn_sqrtf(rbf(vb + 1e-5f))));
}

constexpr int PREP_KGEN = 640, PREP_LN = 128, PREP_WQ = 100, PREP_WO = 100, PREP_WKV = 20, PREP_CS = 11, PREP_KV = 1;
constexpr int PREP_BLOCKS = PREP_KGEN + PREP_LN + PREP_WQ + PREP_WO + PREP_WKV + PREP_CS + PREP_KV;

DI void cexp_rev(float e0, float e1, float f0, float f1, float p, float& er, float& ei) {
  const float mag = __builtin_amdgcn_exp2f(__builtin_fmaf(e1, p, e0));
  const float rev = __builtin_amdgcn_fractf(__builtin_fmaf(f1, p, f0));
  er = mag * __builtin_amdgcn_cosf(rev);
  ei = mag * __builtin_amdgcn_sinf(rev);
}

__global__ __launch_bounds__(256) void prep_kernel(const u16* __restrict__ xu, const u16* __restrict__ lar,
                                                   const u16* __restrict__ aim, const u16* __restrict__ cre,
                                                   const u16* __restrict__ cim, const u16* __restrict__ ldt,
                                                   const u16* __restrict__ wq, const u16* __restrict__ wk,
                                                   const u16* __restrict__ wv, const u16* __restrict__ wo,
                                                   char* __restrict__ ws) {
  __shared__ __attribute__((aligned(16))) char smem[16 * 1296 + 64];
  const int tid = threadIdx.x, lane = tid & 63, w = tid >> 6;
  int bid = blockIdx.x;
  if (bid < PREP_KGEN) {
    const int d = bid;
    f32x4* prm = (f32x4*)smem;
    float* part = (float*)(smem + 2048);
    float* ktab = (float*)(smem + 2048 + 12288);
    if (tid < NS) {
      const float dt = rbf(expf(bf2f(ldt[d])));
      const float are = -rbf(expf(bf2f(lar[d * NS + tid])));
      const float ai = bf2f(aim[d * NS + tid]);
      const float cr = bf2f(cre[d * NS + tid]) * dt, ci = bf2f(cim[d * NS + tid]) * dt;
      const float mx = fmaxf(fabsf(cr), fabsf(ci));
      float lc = -1000.f, ph = 0.f;
      if (mx > 0.f) {
        const float a = cr / mx, b = ci / mx;
        lc = log2f(mx) + 0.5f * log2f(a * a + b * b);
        ph = atan2f(ci, cr) * 0.15915494309189535f;
      }
      f32x4 p = {are * dt * 1.4426950408889634f, ai * dt * 0.15915494309189535f, lc, ph};
      prm[tid] = p;
    }
    __syncthreads();
    const int i = lane & 15, kq = lane >> 4;
    const float A0 = (float)(128 * (i >> 3) + (i & 7));
    const float A1 = (float)(i < 8 ? 256 + 128 * (i >> 2) + 2 * (i & 3) : 512 + 128 * ((i - 8) >> 1) + 4 * (i & 1));
    const float A2 = (float)(i <= 8 ? 1024 + 128 * i : 0);
    const float Bp = (float)(8 * i);
    f32x4 acc0 = {}, acc1 = {}, acc2 = {};
#pragma unroll 2
    for (int t = 0; t < 8; ++t) {
      const f32x4 p = prm[32 * w + 4 * t + kq];
      float br, bi, ar, ai_;
      cexp_rev(0.f, p[0], 0.f, p[1], Bp, br, bi);
      cexp_rev(p[2], p[0], p[3], p[1], A0, ar, ai_);
      acc0 = __builtin_amdgcn_mfma_f32_16x16x4f32(ar, br, acc0, 0, 0, 0);
      acc0 = __builtin_amdgcn_mfma_f32_16x16x4f32(-ai_, bi, acc0, 0, 0, 0);
      cexp_rev(p[2], p[0], p[3], p[1], A1, ar, ai_);
      acc1 = __builtin_amdgcn_mfma_f32_16x16x4f32(ar, br, acc1, 0, 0, 0);
      acc1 = __builtin_amdgcn_mfma_f32_16x16x4f32(-ai_, bi, acc1, 0, 0, 0);
      cexp_rev(p[2], p[0], p[3], p[1], A2, ar, ai_);
      acc2 = __builtin_amdgcn_mfma_f32_16x16x4f32(ar, br, acc2, 0, 0, 0);
      acc2 = __builtin_amdgcn_mfma_f32_16x16x4f32(-ai_, bi, acc2, 0, 0, 0);
    }
#pragma unroll
    for (int r = 0; r < 4; ++r) {
      const int idx = (4 * kq + r) * 16 + i;
      part[(w * 3 + 0) * 256 + idx] = acc0[r];
      part[(w * 3 + 1) * 256 + idx] = acc1[r];
      part[(w * 3 + 2) * 256 + idx] = acc2[r];
    }
    __syncthreads();
    {
      const int r = tid >> 4, b8 = 8 * (tid & 15);
      const int p0 = 128 * (r >> 3) + (r & 7) + b8;
      const int p1 = (r < 8 ? 256 + 128 * (r >> 2) + 2 * (r & 3) : 512 + 128 * ((r - 8) >> 1) + 4 * (r & 1)) + b8;
      const int p2 = 1024 + 128 * r + b8;
      const float k0 = 2.f * (part[0 * 256 + tid] + part[3 * 256 + tid] + part[6 * 256 + tid] + part[9 * 256 + tid]);
      const float k1 = 2.f * (part[1 * 256 + tid] + part[4 * 256 + tid] + part[7 * 256 + tid] + part[10 * 256 + tid]);
      const float k2 = 2.f * (part[2 * 256 + tid] + part[5 * 256 + tid] + part[8 * 256 + tid] + part[11 * 256 + tid]);
      ktab[p0] = k0;
      ktab[r < 8 ? 128 + (p1 >> 1) : 256 + (p1 >> 2)] = k1;
      if (p2 <= 2048) ktab[384 + (p2 >> 3)] = k2;
    }
    __syncthreads();
    float v[8];
#pragma unroll
    for (int e = 0; e < 8; ++e) {
      const int l = 2047 - (8 * tid + e);
      const int lc = l < 1 ? 1 : l;
      const int sh = max(24 - __builtin_clz((unsigned)lc), 0);
      const int add = sh ? ((1 << sh) >> 1) - 1 + ((lc >> sh) & 1) : 0;
      const int idx = l < 1 ? 0 : 128 * sh + ((lc + add) >> sh);
      const float kv = ktab[idx];
      v[e] = l < 0 ? 0.f : kv;
    }
    u16* rgd = (u16*)(ws + WS_RG) + (size_t)d * RG_STRIDE;
    *(u32x4*)(rgd + 8 * tid) = u32x4{pk2(v[0], v[1]), pk2(v[2], v[3]), pk2(v[4], v[5]), pk2(v[6], v[7])};
    if (tid == 0) *(u32x4*)(rgd + 2048) = u32x4{0u, 0u, 0u, 0u};
    return;
  }
  bid -= PREP_KGEN;
  if (bid < PREP_LN) {
    const int l0 = bid * 16;
    const int row = tid >> 4, t16 = tid & 15;
    const u16* src = xu + (size_t)(l0 + row) * D;
    u32x4 v[5];
#pragma unroll
    for (int i = 0; i < 5; ++i) v[i] = *(const u32x4*)(src + (t16 + 16 * i) * 8);
    float s = 0.f;
#pragma unroll
    for (int i = 0; i < 5; ++i) {
#pragma unroll
      for (int j = 0; j < 4; ++j) s += bflo(v[i][j]) + bfhi(v[i][j]);
      *(u32x4*)(smem + row * 1296 + (t16 + 16 * i) * 16) = v[i];
    }
    s += __shfl_xor(s, 1); s += __shfl_xor(s, 2); s += __shfl_xor(s, 4); s += __shfl_xor(s, 8);
    const float mean = s * (1.0f / D);
    float m2 = 0.f;
#pragma unroll
    for (int i = 0; i < 5; ++i)
#pragma unroll
      for (int j = 0; j < 4; ++j) {
        float a = bflo(v[i][j]) - mean, b = bfhi(v[i][j]) - mean;
        m2 += a * a + b * b;
      }
    m2 += __shfl_xor(m2, 1); m2 += __shfl_xor(m2, 2); m2 += __shfl_xor(m2, 4); m2 += __shfl_xor(m2, 8);
    if (t16 == 0) {
      float mb, sb;
      ln_params(s, m2, mb, sb);
      f32x2 o = {mb, sb};
      *(f32x2*)(ws + WS_ST0 + (size_t)(l0 + row) * 8) = o;
    }
    __syncthreads();
    u16* xt = (u16*)(ws + WS_XT0);
    const int g = lane >> 4, q = (lane & 15) >> 2, p = lane & 3, i16 = lane & 15;
#pragma unroll
    for (int it = 0; it < 5; ++it) {
      const int cb = (it * 4 + w) * 2 + (g >> 1);
      const char* base = smem + (8 * (g & 1) + q) * 1296 + cb * 32 + 8 * p;
      s16x4 lo = tr_read(base);
      s16x4 hi = tr_read(base + 4 * 1296);
      u32x2 a = __builtin_bit_cast(u32x2, lo), b = __builtin_bit_cast(u32x2, hi);
      u32x4 o = {a[0], a[1], b[0], b[1]};
      *(u32x4*)(xt + (size_t)(cb * 16 + i16) * LT + l0 + 8 * (g & 1)) = o;
    }
    return;
  }
  bid -= PREP_LN;
  if (bid < PREP_WQ + PREP_WO) {
    const bool isq = bid < PREP_WQ;
    const int t = isq ? bid : bid - PREP_WQ;
    const int ks = t / 5, nb = t % 5;
    const u16* src = (isq ? wq : wo) + (size_t)(32 * ks) * D + 128 * nb;
#pragma unroll
    for (int i = 0; i < 2; ++i) {
      const int c = tid + 256 * i, r = c >> 4, cc = c & 15;
      *(u32x4*)(smem + r * 272 + cc * 16) = *(const u32x4*)(src + (size_t)r * D + cc * 8);
    }
    __syncthreads();
    const int g = lane >> 4, q = (lane & 15) >> 2, p = lane & 3;
#pragma unroll
    for (int i = 0; i < 2; ++i) {
      const int ntl = w * 2 + i;
      const char* base = smem + (8 * g + q) * 272 + ntl * 32 + 8 * p;
      s16x4 lo = tr_read(base);
      s16x4 hi = tr_read(base + 4 * 272);
      u32x2 a = __builtin_bit_cast(u32x2, lo), b = __builtin_bit_cast(u32x2, hi);
      u32x4 o = {a[0], a[1], b[0], b[1]};
      *(u32x4*)(ws + (isq ? WS_WQ : WS_WO) + (size_t)((8 * nb + ntl) * 20 + ks) * 1024 + lane * 16) = o;
    }
    return;
  }
  bid -= PREP_WQ + PREP_WO;
  if (bid < PREP_WKV) {
    const int ks = bid, nt = 40 + w;
    const int n = (lane & 15), k0 = 32 * ks + 8 * (lane >> 4);
    const u16* src = (w < 2) ? wk : wv;
    const int col = 16 * (w & 1) + n;
    const bool valid = col < DH;
    unsigned e[8];
#pragma unroll
    for (int j = 0; j < 8; ++j) e[j] = valid ? (unsigned)src[(size_t)(k0 + j) * DH + col] : 0u;
    u32x4 o = {e[0] | (e[1] << 16), e[2] | (e[3] << 16), e[4] | (e[5] << 16), e[6] | (e[7] << 16)};
    *(u32x4*)(ws + WS_WQ + (size_t)(nt * 20 + ks) * 1024 + lane * 16) = o;
    return;
  }
  bid -= PREP_WKV;
  if (bid < PREP_CS) {
    float* red = (float*)smem;
    if (bid < 10) {
      const int cg = tid & 7, kg = tid >> 3;
      const u16* src = wq + (size_t)(20 * kg) * D + 64 * bid + 8 * cg;
      float sm[8];
#pragma unroll
      for (int e = 0; e < 8; ++e) sm[e] = 0.f;
#pragma unroll
      for (int k = 0; k < 20; ++k) {
        const u32x4 v = *(const u32x4*)(src + (size_t)k * D);
#pragma unroll
        for (int j = 0; j < 4; ++j) { sm[2 * j] += bflo(v[j]); sm[2 * j + 1] += bfhi(v[j]); }
      }
#pragma unroll
      for (int e = 0; e < 8; ++e) red[kg * 64 + 8 * cg + e] = sm[e];
    } else {
      for (int c = tid; c < 32 * 64; c += 256) red[c] = 0.f;
      __syncthreads();
      for (int it = tid; it < 640; it += 256) {
        const int kg = it / 20, cp = it % 20;
        const u16* src = (cp < 10 ? wk : wv) + (size_t)(20 * kg) * DH + 2 * (cp % 10);
        float a = 0.f, b = 0.f;
#pragma unroll
        for (int k = 0; k < 20; ++k) {
          const unsigned v = *(const unsigned*)(src + (size_t)k * DH);
          a += bflo(v); b += bfhi(v);
        }
        const int col = (cp < 10 ? 0 : 32) + 2 * (cp % 10);
        red[kg * 64 + col] = a; red[kg * 64 + col + 1] = b;
      }
    }
    __syncthreads();
    if (tid < 64) {
      float t = 0.f;
#pragma unroll
      for (int kg = 0; kg < 32; ++kg) t += red[kg * 64 + tid];
      ((float*)(ws + WS_CS))[64 * bid + tid] = t;
    }
    return;
  }
  bid -= PREP_CS;
  {
    if (bid == 0) {
      for (int c = tid; c < 3 * 64 * 32; c += 256) ((unsigned*)(ws + WS_CNT))[c] = 0u;
    }
    for (int c = bid * 256 + tid; c < 64 * 4; c += PREP_KV * 256) {
      u32x4 o = {0x3f803f80u, 0x3f803f80u, 0x3f803f80u, 0x3f803f80u};
      *(u32x4*)(ws + WS_KV + (size_t)(c >> 2) * TS + TV + (c & 3) * TVB + DH * 16) = o;
    }
  }
}

constexpr int CS = 4160;
__global__ __launch_bounds__(256) void conv_kernel(const u16* __restrict__ xt, const f32x4* __restrict__ stats,
                                                   const u16* __restrict__ rg, const u16* __restrict__ dskip,
                                                   u16* __restrict__ xout) {
  __shared__ __attribute__((aligned(16))) char smem[8 * CS + 96 * 80];
  char* KC = smem;
  char* U = smem + 8 * CS;
  const int tid = threadIdx.x, lane = tid & 63, w = tid >> 6;
  const int d = blockIdx.x;
  const u16* rgd = rg + (size_t)d * RG_STRIDE;
  const u32x4 xv = *(const u32x4*)(xt + (size_t)d * LT + 8 * tid);
  float mb[8], sb[8];
#pragma unroll
  for (int e = 0; e < 4; ++e) {
    const f32x4 s = stats[4 * tid + e];
    mb[2 * e] = s[0]; sb[2 * e] = s[1]; mb[2 * e + 1] = s[2]; sb[2 * e + 1] = s[3];
  }
  const u32x4 r0 = *(const u32x4*)(rgd + 8 * tid);
  const u32x4 r1 = *(const u32x4*)(rgd + 8 * tid + 8);
  const float dsk = bf2f(dskip[d]);
  {
    unsigned wv[8] = {r0[0], r0[1], r0[2], r0[3], r1[0], r1[1], r1[2], r1[3]};
#pragma unroll
    for (int s = 0; s < 8; ++s) {
      u32x4 o;
#pragma unroll
      for (int k = 0; k < 4; ++k) {
        if (s & 1) o[k] = __builtin_amdgcn_alignbit(wv[(s >> 1) + k + 1], wv[(s >> 1) + k], 16);
        else o[k] = wv[(s >> 1) + k];
      }
      *(u32x4*)(KC + s * CS + tid * 16) = o;
    }
    if (tid < 32) {
      u32x4 z = {0u, 0u, 0u, 0u};
      *(u32x4*)(KC + (tid >> 2) * CS + (256 + (tid & 3)) * 16) = z;
    }
  }
  float xf[8], uf[8];
#pragma unroll
  for (int j = 0; j < 4; ++j) { xf[2 * j] = bflo(xv[j]); xf[2 * j + 1] = bfhi(xv[j]); }
#pragma unroll
  for (int e = 0; e < 8; ++e) uf[e] = rbf(rbf(xf[e] - mb[e]) * sb[e]);
  {
    u32x4 o = {pk2(uf[0], uf[1]), pk2(uf[2], uf[3]), pk2(uf[4], uf[5]), pk2(uf[6], uf[7])};
    *(u32x4*)(U + ((tid >> 2) + 32) * 80 + (tid & 3) * 16) = o;
    if (tid < 128) {
      u32x4 z = {0u, 0u, 0u, 0u};
      *(u32x4*)(U + (tid >> 2) * 80 + (tid & 3) * 16) = z;
    }
  }
  __syncthreads();
  const int b = lane & 31, h = lane >> 5;
  const char* kbase = KC + (7 - (b & 7)) * CS + 16 * (255 - (b >> 3) + h);
  const char* ubase = U + (b + 32) * 80 + 16 * h;
  f32x16 acc0 = {}, acc1 = {};
  const int c0 = w == 0 ? 0 : (w == 1 ? 12 : 24);
  const int nboth = w < 2 ? 12 : (w == 2 ? 8 : 0);
  const int c1 = w == 3 ? 40 : 32, nsing = w < 2 ? 0 : (w == 2 ? 8 : 24);
  if (nboth) {
    const char* kp = kbase - 64 * c0;
    const char* up = ubase - 80 * c0;
    bf16x8 kf0 = *(const bf16x8*)(kp), kf1 = *(const bf16x8*)(kp + 32);
    bf16x8 a0 = *(const bf16x8*)(up), a1 = *(const bf16x8*)(up + 32);
    bf16x8 b0 = *(const bf16x8*)(up + 2560), b1 = *(const bf16x8*)(up + 2560 + 32);
    for (int c = 0; c < nboth; ++c) {
      kp -= 64; up -= 80;
      const bf16x8 nk0 = *(const bf16x8*)(kp), nk1 = *(const bf16x8*)(kp + 32);
      const bf16x8 na0 = *(const bf16x8*)(up), na1 = *(const bf16x8*)(up + 32);
      const bf16x8 nb0 = *(const bf16x8*)(up + 2560), nb1 = *(const bf16x8*)(up + 2560 + 32);
      acc0 = __builtin_amdgcn_mfma_f32_32x32x16_bf16(a0, kf0, acc0, 0, 0, 0);
      acc1 = __builtin_amdgcn_mfma_f32_32x32x16_bf16(b0, kf0, acc1, 0, 0, 0);
      acc0 = __builtin_amdgcn_mfma_f32_32x32x16_bf16(a1, kf1, acc0, 0, 0, 0);
      acc1 = __builtin_amdgcn_mfma_f32_32x32x16_bf16(b1, kf1, acc1, 0, 0, 0);
      kf0 = nk0; kf1 = nk1; a0 = na0; a1 = na1; b0 = nb0; b1 = nb1;
    }
  }
  if (nsing) {
    const char* kp = kbase - 64 * c1;
    const char* up = ubase + 80 * (32 - c1);
    bf16x8 kf0 = *(const bf16x8*)(kp), kf1 = *(const bf16x8*)(kp + 32);
    bf16x8 b0 = *(const bf16x8*)(up), b1 = *(const bf16x8*)(up + 32);
    for (int c = 0; c < nsing; ++c) {
      const bool more = c + 1 < nsing;
      kp -= more ? 64 : 0; up -= more ? 80 : 0;
      const bf16x8 nk0 = *(const bf16x8*)(kp), nk1 = *(const bf16x8*)(kp + 32);
      const bf16x8 nb0 = *(const bf16x8*)(up), nb1 = *(const bf16x8*)(up + 32);
      acc1 = __builtin_amdgcn_mfma_f32_32x32x16_bf16(b0, kf0, acc1, 0, 0, 0);
      acc1 = __builtin_amdgcn_mfma_f32_32x32x16_bf16(b1, kf1, acc1, 0, 0, 0);
      kf0 = nk0; kf1 = nk1; b0 = nb0; b1 = nb1;
    }
  }
  __syncthreads();
  float* part = (float*)smem;
#pragma unroll
  for (int r = 0; r < 16; ++r) {
    const int a = crow(r, h);
    part[w * 2048 + 32 * a + b] = acc0[r];
    part[w * 2048 + 1024 + 32 * a + b] = acc1[r];
  }
  __syncthreads();
  float y[8];
#pragma unroll
  for (int e = 0; e < 8; ++e) y[e] = 0.f;
#pragma unroll
  for (int ww = 0; ww < 4; ++ww) {
    const f32x4 p0 = *(const f32x4*)(part + ww * 2048 + 8 * tid);
    const f32x4 p1 = *(const f32x4*)(part + ww * 2048 + 8 * tid + 4);
#pragma unroll
    for (int e = 0; e < 4; ++e) { y[e] += p0[e]; y[4 + e] += p1[e]; }
  }
  float o[8];
#pragma unroll
  for (int e = 0; e < 8; ++e) o[e] = xf[e] + rbf(rbf(y[e]) + rbf(uf[e] * dsk));
  u32x4 ov = {pk2(o[0], o[1]), pk2(o[2], o[3]), pk2(o[4], o[5]), pk2(o[6], o[7])};
  *(u32x4*)(xout + (size_t)d * LT + 8 * tid) = ov;
}

__global__ __launch_bounds__(512, 2) void qkv_kernel(const u16* __restrict__ xt, const char* __restrict__ wf,
                                                     const float* __restrict__ csum, u16* __restrict__ Q,
                                                     char* __restrict__ kvf) {
  __shared__ __attribute__((aligned(16))) char smem[640 * 128 + 512 + 6 * 5 * 1024];
  f32x2* lnp = (f32x2*)(smem + 640 * 128);
  const int tid = threadIdx.x, lane = tid & 63, w = tid >> 6;
  const int idx = blockIdx.x >> 3;
  const int rt = (idx >> 3) * 8 + (blockIdx.x & 7), ns = idx & 7;
  const int l0 = 64 * rt;
  {
    const int dr = lane >> 3, c = lane & 7;
    const int sw = ((dr >> 1) & 1) | ((w & 1) << 1);
#pragma unroll
    for (int i = 0; i < 10; ++i) {
      const int piece = i * 8 + w;
      const int d = 8 * piece + dr;
      const u16* src = xt + (size_t)d * LT + l0 + 8 * (c ^ (sw << 1));
      __builtin_amdgcn_global_load_lds((const __attribute__((address_space(1))) void*)src,
                                       (__attribute__((address_space(3))) void*)(smem + piece * 1024), 16, 0, 0);
    }
  }
  const int P = w % 3, Hh = w / 3;
  const int ntp = ns * 6 + 2 * P;
  const bool active = (w < 6) && (ntp < 44);
  const char* wbase = wf + (size_t)((active ? ntp : 0) * 20 + 10 * Hh) * 1024 + lane * 16;
  bf16x8 w0[10], w1[10];
  const f32x4 accc = *(const f32x4*)(csum + 16 * ((active ? ntp : 0) + Hh % 2) + 4 * (lane >> 4));
  if (active) {
#pragma unroll
    for (int ks = 0; ks < 10; ++ks) w0[ks] = *(const bf16x8*)(wbase + ks * 1024);
#pragma unroll
    for (int ks = 0; ks < 10; ++ks) w1[ks] = *(const bf16x8*)(wbase + 20 * 1024 + ks * 1024);
  }
  __syncthreads();
  const int g = lane >> 4, q = (lane & 15) >> 2, p = lane & 3;
  const int sw = ((q >> 1) & 1) | ((g & 1) << 1);
  const char* ub = smem + (8 * g + q) * 128 + 8 * p;
  const u32x4 onesu = {0x3f803f80u, 0x3f803f80u, 0x3f803f80u, 0x3f803f80u};
  const bf16x8 ones = __builtin_bit_cast(bf16x8, onesu);
  f32x4 acc[4];
  f32x4* xch = (f32x4*)(smem + 640 * 128 + 512);
#pragma unroll
  for (int a = 0; a < 4; ++a) acc[a] = f32x4{};
  if (active) {
    f32x4 bcc[4];
#pragma unroll
    for (int a = 0; a < 4; ++a) bcc[a] = f32x4{};
    const char* ubh = ub + Hh * 10 * 4096;
    bf16x8 uf[2][8];
    auto rd2 = [&](int kp, bf16x8* dst) {
#pragma unroll
      for (int kk = 0; kk < 2; ++kk)
#pragma unroll
        for (int lt = 0; lt < 4; ++lt) {
          const char* pp = ubh + (2 * kp + kk) * 4096 + ((lt ^ sw) << 5);
          s16x4 a0 = tr_read(pp), a1 = tr_read(pp + 512);
          dst[kk * 4 + lt] = __builtin_bit_cast(bf16x8, __builtin_shufflevector(a0, a1, 0, 1, 2, 3, 4, 5, 6, 7));
        }
    };
    rd2(0, uf[0]);
#pragma unroll
    for (int kp = 0; kp < 5; ++kp) {
      __builtin_amdgcn_sched_barrier(0);
      if (kp + 1 < 5) rd2(kp + 1, uf[(kp + 1) & 1]);
      __builtin_amdgcn_sched_barrier(0);
#pragma unroll
      for (int kk = 0; kk < 2; ++kk) {
        const int ks = 2 * kp + kk;
#pragma unroll
        for (int lt = 0; lt < 4; ++lt) {
          acc[lt] = __builtin_amdgcn_mfma_f32_16x16x32_bf16(w0[ks], uf[kp & 1][kk * 4 + lt], acc[lt], 0, 0, 0);
          bcc[lt] = __builtin_amdgcn_mfma_f32_16x16x32_bf16(w1[ks], uf[kp & 1][kk * 4 + lt], bcc[lt], 0, 0, 0);
        }
      }
    }
    if (Hh) {
#pragma unroll
      for (int a = 0; a < 4; ++a) { xch[(w * 5 + a) * 64 + lane] = acc[a]; acc[a] = bcc[a]; }
    } else {
#pragma unroll
      for (int a = 0; a < 4; ++a) xch[(w * 5 + a) * 64 + lane] = bcc[a];
    }
  } else if (w >= 6) {
    bf16x8 uf[2][8];
    auto rd4 = [&](int kp, bf16x8* dst) {
#pragma unroll
      for (int kk = 0; kk < 4; ++kk)
#pragma unroll
        for (int j = 0; j < 2; ++j) {
          const char* pp = ub + (4 * kp + kk) * 4096 + (((2 * (w - 6) + j) ^ sw) << 5);
          s16x4 a0 = tr_read(pp), a1 = tr_read(pp + 512);
          dst[kk * 2 + j] = __builtin_bit_cast(bf16x8, __builtin_shufflevector(a0, a1, 0, 1, 2, 3, 4, 5, 6, 7));
        }
    };
    rd4(0, uf[0]);
#pragma unroll
    for (int kp = 0; kp < 5; ++kp) {
      __builtin_amdgcn_sched_barrier(0);
      if (kp + 1 < 5) rd4(kp + 1, uf[(kp + 1) & 1]);
      __builtin_amdgcn_sched_barrier(0);
#pragma unroll
      for (int kk = 0; kk < 4; ++kk)
#pragma unroll
        for (int j = 0; j < 2; ++j) {
          const bf16x8 u0 = uf[kp & 1][kk * 2 + j];
          acc[j] = __builtin_amdgcn_mfma_f32_16x16x32_bf16(ones, u0, acc[j], 0, 0, 0);
          acc[2 + j] = __builtin_amdgcn_mfma_f32_16x16x32_bf16(u0, u0, acc[2 + j], 0, 0, 0);
        }
    }
    const int li = lane & 15;
    if (g == (li >> 2)) {
#pragma unroll
      for (int j = 0; j < 2; ++j) {
        const float sx = acc[j][0];
        const f32x4 gm = acc[2 + j];
        const int r = li & 3;
        const float sxx = r == 0 ? gm[0] : (r == 1 ? gm[1] : (r == 2 ? gm[2] : gm[3]));
        float mb, sb;
        ln_params(sx, fmaxf(sxx - sx * sx * (1.0f / D), 0.f), mb, sb);
        f32x2 o = {mb, sb};
        lnp[16 * (2 * (w - 6) + j) + li] = o;
      }
    }
  }
  __syncthreads();
  if (!active) return;
  const int nt = ntp + Hh;
  {
    const int pw = Hh ? w - 3 : w + 3;
#pragma unroll
    for (int a = 0; a < 4; ++a) acc[a] += xch[(pw * 5 + a) * 64 + lane];
  }
#pragma unroll
  for (int lt = 0; lt < 4; ++lt) {
    const f32x2 pr = lnp[16 * lt + (lane & 15)];
#pragma unroll
    for (int e = 0; e < 4; ++e) acc[lt][e] = (acc[lt][e] - pr[0] * accc[e]) * pr[1];
  }
  const float qscale = 0.22360679774997896f * 1.4426950408889634f;
#pragma unroll
  for (int lt = 0; lt < 4; ++lt) {
    const int l = l0 + 16 * lt + (lane & 15);
    const f32x4 r = acc[lt];
    if (nt < 40) {
      u32x2 o = {pk2(r[0] * qscale, r[1] * qscale), pk2(r[2] * qscale, r[3] * qscale)};
      *(u32x2*)(Q + (size_t)l * D + 16 * nt + 4 * g) = o;
    } else if (nt < 42) {
      const int r32 = l & 31;
      u32x2 o = {pk2(r[0], r[1]), pk2(r[2], r[3])};
      if (nt == 40) *(u32x2*)(kvf + (size_t)(l >> 5) * TS + (r32 + 32 * (g >> 1)) * 16 + 8 * (g & 1)) = o;
      else if (g == 0) *(u32x2*)(kvf + (size_t)(l >> 5) * TS + TK1 + r32 * 8) = o;
    } else {
      const int r32 = l & 31;
      const int s_ = r32 >> 4, jh = (r32 >> 3) & 1, hh = (r32 >> 2) & 1, jl = r32 & 3;
      const int j = 4 * jh + jl;
#pragma unroll
      for (int e = 0; e < 4; ++e) {
        const int dv = 16 * (nt - 42) + 4 * g + e;
        if (dv < DH) {
          u16 bv = (u16)(pk2(r[e], 0.f) & 0xffffu);
          *(u16*)(kvf + (size_t)(l >> 5) * TS + TV + (2 * s_ + hh) * TVB + dv * 16 + 2 * j) = bv;
        }
      }
    }
  }
}

constexpr int ACH = 11;
constexpr int CBUF = 29 * 1024;
constexpr int WLK = 10;
struct KVOff { int k0, k1, v0; };
DI KVOff kv_offsets(int lane) {
  const int r = lane & 31, h = lane >> 5;
  KVOff o;
  o.k0 = lane * 16;
  o.k1 = TK1 + r * 8;
  o.v0 = TV + h * TVB + (r < DH ? r : DH) * 16;
  return o;
}
DI void kv_load_k(const char* tb, const KVOff& f, bf16x8& k0, bf16x8& k1) {
  k0 = *(const bf16x8*)(tb + f.k0);
  const u32x2 t = *(const u32x2*)(tb + f.k1);
  k1 = __builtin_bit_cast(bf16x8, u32x4{t[0], t[1], 0u, 0u});
}
DI void kv_load_v(const char* tb, const KVOff& f, bf16x8& v0, bf16x8& v1) {
  v0 = *(const bf16x8*)(tb + f.v0);
  v1 = *(const bf16x8*)(tb + f.v0 + 2 * TVB);
}
DI void attn_safe_tile(const char* tb, const KVOff& f, const bf16x8& qf0, const bf16x8& qf1, f32x16& o, f32x16& negm,
                       bool first) {
  bf16x8 k0, k1, v0, v1;
  kv_load_k(tb, f, k0, k1);
  kv_load_v(tb, f, v0, v1);
  f32x16 s = __builtin_amdgcn_mfma_f32_32x32x16_bf16(k0, qf0, negm, 0, 0, 0);
  s = __builtin_amdgcn_mfma_f32_32x32x16_bf16(k1, qf1, s, 0, 0, 0);
  float tm = s[0];
#pragma unroll
  for (int r = 1; r < 16; ++r) tm = fmaxf(tm, s[r]);
  tm = fmaxf(tm, __shfl_xor(tm, 32));
  const float delta = first ? tm : fmaxf(tm, 0.f);
  const float sc = first ? 0.f : __builtin_amdgcn_exp2f(-delta);
#pragma unroll
  for (int r = 0; r < 16; ++r) { o[r] *= sc; negm[r] -= delta; }
  u32x4 pa, pb;
#pragma unroll
  for (int r = 0; r < 4; ++r) {
    pa[r] = pk2(__builtin_amdgcn_exp2f(s[2 * r] - delta), __builtin_amdgcn_exp2f(s[2 * r + 1] - delta));
    pb[r] = pk2(__builtin_amdgcn_exp2f(s[8 + 2 * r] - delta), __builtin_amdgcn_exp2f(s[8 + 2 * r + 1] - delta));
  }
  o = __builtin_amdgcn_mfma_f32_32x32x16_bf16(v0, __builtin_bit_cast(bf16x8, pa), o, 0, 0, 0);
  o = __builtin_amdgcn_mfma_f32_32x32x16_bf16(v1, __builtin_bit_cast(bf16x8, pb), o, 0, 0, 0);
}
#define SBAR() __builtin_amdgcn_sched_barrier(0)
#define EXP4(P, I, S, B)                                                                   \
  P[I] = pk2(__builtin_amdgcn_exp2f(S[B]), __builtin_amdgcn_exp2f(S[B + 1]));              \
  P[I + 1] = pk2(__builtin_amdgcn_exp2f(S[B + 2]), __builtin_amdgcn_exp2f(S[B + 3]));
struct AttnState {
  f32x16 o, s_cur;
  bf16x8 kA0, kA1;
  bf16x8 vA0, vA1;
  bf16x8 vB0, vB1;
  bf16x8 v1p;
  u32x4 pbp;
};
template <int N, bool FIRST, bool LAST, typename F>
DI void attn_chunk(AttnState& st, const char* cur, const char* nxt, const KVOff& f, const bf16x8& qf0,
                   const bf16x8& qf1, const f32x16& negm, F sync) {
  f32x16 s_nxt;
  bf16x8 kB0, kB1, vC0, vC1;
  u32x4 pa, pb;
#pragma unroll
  for (int i = 0; i < N; ++i) {
    const bool has1 = !(LAST && i + 1 >= N);
    const bool has2 = !(LAST && i + 2 >= N);
    SBAR();
    if (!LAST && i == N - 2) {
      asm volatile("" : "+v"(st.o), "+v"(st.pbp), "+v"(st.s_cur));
      sync();
    }
    if (has1) s_nxt = __builtin_amdgcn_mfma_f32_32x32x16_bf16(st.kA0, qf0, negm, 0, 0, 0);
    if (has2) {
      const char* tp = (i + 2 < N) ? cur + (i + 2) * TS : nxt + (i + 2 - N) * TS;
      kv_load_k(tp, f, kB0, kB1);
      kv_load_v(tp, f, vC0, vC1);
    }
    SBAR();
    EXP4(pa, 0, st.s_cur, 0);
    SBAR();
    if (!(FIRST && i == 0)) st.o = __builtin_amdgcn_mfma_f32_32x32x16_bf16(st.v1p, __builtin_bit_cast(bf16x8, st.pbp), st.o, 0, 0, 0);
    SBAR();
    EXP4(pa, 2, st.s_cur, 4);
    SBAR();
    if (has1) s_nxt = __builtin_amdgcn_mfma_f32_32x32x16_bf16(st.kA1, qf1, s_nxt, 0, 0, 0);
    SBAR();
    EXP4(pb, 0, st.s_cur, 8);
    SBAR();
    st.o = __builtin_amdgcn_mfma_f32_32x32x16_bf16(st.vA0, __builtin_bit_cast(bf16x8, pa), st.o, 0, 0, 0);
    SBAR();
    EXP4(pb, 2, st.s_cur, 12);
    st.pbp = pb; st.v1p = st.vA1;
    st.s_cur = s_nxt; st.kA0 = kB0; st.kA1 = kB1;
    st.vA0 = st.vB0; st.vA1 = st.vB1; st.vB0 = vC0; st.vB1 = vC1;
  }
  SBAR();
  if (LAST) st.o = __builtin_amdgcn_mfma_f32_32x32x16_bf16(st.v1p, __builtin_bit_cast(bf16x8, st.pbp), st.o, 0, 0, 0);
}

typedef __attribute__((address_space(1))) unsigned gu32;
typedef __attribute__((address_space(1))) unsigned long long gu64;
#define RLX_AGENT __ATOMIC_RELAXED, __HIP_MEMORY_SCOPE_AGENT

template <bool FINAL>
__global__ __launch_bounds__(512, 2) void attn_kernel(const u16* __restrict__ Q, const char* __restrict__ kvf,
                                                      u16* O, const char* __restrict__ wf,
                                                      const u16* __restrict__ xres, u16* __restrict__ xout,
                                                      f32x2* pstats, f32x2* __restrict__ fstats, unsigned* cnt) {
  __shared__ __attribute__((aligned(16))) char smem[2 * CBUF + 10 * WLK * 1024];
  const int tid = threadIdx.x, lane = tid & 63, w = tid >> 6;
  const int hq = lane & 31, h = lane >> 5;
  const int idx = blockIdx.x >> 3;
  const int rt = (idx >> 2) * 8 + (blockIdx.x & 7), ns = idx & 3;
  const int row = 32 * rt + 8 * ns + w;
  auto stage = [&](int chunk, int buf) {
    const int np = chunk < 5 ? 29 : 24;
#pragma unroll
    for (int i = 0; i < 4; ++i) {
      const int pc = chunk < 2 ? min(w + 8 * i, np - 1) : w + 8 * i;
      if (pc < np) {
        const char* src = kvf + (size_t)chunk * ACH * TS + pc * 1024 + lane * 16;
        __builtin_amdgcn_global_load_lds((const __attribute__((address_space(1))) void*)src,
                                         (__attribute__((address_space(3))) void*)(smem + buf * CBUF + pc * 1024), 16, 0, 0);
      }
    }
  };
  stage(0, 0);
  bf16x8 qf0, qf1;
  {
    const u16* qp = Q + (size_t)row * D + hq * DH;
    u32x2 a = *(const u32x2*)(qp + 8 * h), b = *(const u32x2*)(qp + 8 * h + 4);
    u32x4 t = {a[0], a[1], b[0], b[1]};
    qf0 = __builtin_bit_cast(bf16x8, t);
    u32x2 c = *(const u32x2*)(qp + 16);
    u32x4 t1 = {h ? 0u : c[0], h ? 0u : c[1], 0u, 0u};
    qf1 = __builtin_bit_cast(bf16x8, t1);
  }
  stage(1, 1);
  f32x16 negm = {};
  const int l0 = 32 * rt;
  const int g = lane >> 4, i16 = lane & 15;
  const int nt0 = ns * 10 + 2 * (w < 5 ? w : 0);
  bf16x8 w0[20 - WLK], w1[20 - WLK];
  u32x2 xr[2][2];
  u16 xs[2][2][4];
  asm volatile("s_waitcnt vmcnt(4)" ::: "memory");
  __builtin_amdgcn_s_barrier();
  asm volatile("" ::: "memory");
  const char* buf0 = smem;
  const char* buf1 = smem + CBUF;
  const KVOff kvo = kv_offsets(lane);
  AttnState st;
  if (__builtin_amdgcn_readfirstlane(threadIdx.x) >= 256) __builtin_amdgcn_s_setprio(1);
  {
    bf16x8 k0, k1;
    kv_load_k(buf0, kvo, k0, k1);
    kv_load_v(buf0, kvo, st.vA0, st.vA1);
    kv_load_k(buf0 + TS, kvo, st.kA0, st.kA1);
    kv_load_v(buf0 + TS, kvo, st.vB0, st.vB1);
    f32x16 s = __builtin_amdgcn_mfma_f32_32x32x16_bf16(k0, qf0, negm, 0, 0, 0);
    s = __builtin_amdgcn_mfma_f32_32x32x16_bf16(k1, qf1, s, 0, 0, 0);
    float tm = s[0];
#pragma unroll
    for (int r = 1; r < 16; ++r) tm = fmaxf(tm, s[r]);
    tm = fmaxf(tm, __shfl_xor(tm, 32));
#pragma unroll
    for (int r = 0; r < 16; ++r) { st.s_cur[r] = s[r] - tm; negm[r] = -tm; st.o[r] = 0.f; }
    st.v1p = st.vA1; st.pbp = u32x4{0u, 0u, 0u, 0u};
  }
  const char* wbase = wf + (size_t)(nt0 * 20 + WLK) * 1024 + lane * 16;
  auto sync_stage = [&](int ch) {
    asm volatile("s_waitcnt vmcnt(0) lgkmcnt(0)" ::: "memory");
    __builtin_amdgcn_s_barrier();
    asm volatile("" ::: "memory");
    if (ch + 2 < 6) stage(ch + 2, ch & 1);
    if (ch < 4) {
      for (int f = ch * 25 + w; f < (ch + 1) * 25; f += 8) {
        const int ntl = f / WLK, ks = f % WLK;
        const char* src = wf + (size_t)((ns * 10 + ntl) * 20 + ks) * 1024 + lane * 16;
        __builtin_amdgcn_global_load_lds((const __attribute__((address_space(1))) void*)src,
                                         (__attribute__((address_space(3))) void*)(smem + 2 * CBUF + f * 1024), 16, 0, 0);
      }
    }
  };
  attn_chunk<ACH, true, false>(st, buf0, buf1, kvo, qf0, qf1, negm, [&]() { sync_stage(0); });
  for (int ch = 1; ch < 3; ++ch) {
    const char* cur = (ch & 1) ? buf1 : buf0;
    const char* nxt = (ch & 1) ? buf0 : buf1;
    attn_chunk<ACH, false, false>(st, cur, nxt, kvo, qf0, qf1, negm, [&]() { sync_stage(ch); });
  }
  attn_chunk<ACH, false, false>(st, buf1, buf0, kvo, qf0, qf1, negm, [&]() {
    sync_stage(3);
    if (w < 5) {
#pragma unroll
      for (int ks = 0; ks < 20 - WLK; ++ks) w0[ks] = *(const bf16x8*)(wbase + ks * 1024);
    }
  });
  attn_chunk<ACH, false, false>(st, buf0, buf1, kvo, qf0, qf1, negm, [&]() {
    sync_stage(4);
    if (w < 5) {
#pragma unroll
      for (int ks = 0; ks < 20 - WLK; ++ks) w1[ks] = *(const bf16x8*)(wbase + 20 * 1024 + ks * 1024);
#pragma unroll
      for (int a = 0; a < 2; ++a)
#pragma unroll
        for (int lt = 0; lt < 2; ++lt) {
          if (FINAL) {
#pragma unroll
            for (int e = 0; e < 4; ++e) xs[a][lt][e] = xres[(size_t)(16 * (nt0 + a) + 4 * g + e) * LT + l0 + 16 * lt + i16];
          } else {
            xr[a][lt] = *(const u32x2*)(xres + (size_t)(16 * (nt0 + a) + i16) * LT + l0 + 16 * lt + 4 * g);
          }
        }
    }
  });
  attn_chunk<9, false, true>(st, buf1, buf0, kvo, qf0, qf1, negm, [&]() {});
  __builtin_amdgcn_s_setprio(0);
  f32x16 o = st.o;
  {
    float amax = 0.f;
#pragma unroll
    for (int r = 0; r < 12; ++r) amax = fmaxf(amax, fabsf(o[r]));
    if (__builtin_expect(__any(!(amax < 1.0e30f)), 0)) {
      o = f32x16{};
      negm = f32x16{};
      for (int t = 0; t < 64; ++t) attn_safe_tile(kvf + (size_t)t * TS, kvo, qf0, qf1, o, negm, t == 0);
    }
  }
  __syncthreads();
  {
    float lsum = o[8];
    lsum = __shfl(lsum, hq + 32);
    const float inv = 1.0f / lsum;
#pragma unroll
    for (int g = 0; g < 3; ++g) {
      const int dv = 8 * g + 4 * h;
      if (dv < DH) {
        const int k = hq * DH + dv;
        u32x2 ov = {pk2(o[4 * g] * inv, o[4 * g + 1] * inv), pk2(o[4 * g + 2] * inv, o[4 * g + 3] * inv)};
        *(u32x2*)(smem + ((k >> 3) * 8 + w) * 16 + (k & 7) * 2) = ov;
      }
    }
    __syncthreads();
    const __amdgpu_buffer_rsrc_t rs = __builtin_amdgcn_make_buffer_rsrc((void*)O, 0, L * D * 2, 0x00020000);
    const unsigned obase = (unsigned)((row >> 4) * 20480 + (8 * (ns & 1)) * 16);
#pragma unroll
    for (int i = 0; i < 2; ++i) {
      const int c = tid + 512 * i;
      if (c < 640) {
        const u32x4 t = *(const u32x4*)(smem + c * 16);
        __builtin_amdgcn_raw_buffer_store_b128(t, rs, obase + (unsigned)((c >> 3) * 256 + (c & 7) * 16), 0, 16);
      }
    }
  }
  asm volatile("" ::: "memory");
  asm volatile("s_waitcnt vmcnt(0)" ::: "memory");
  __builtin_amdgcn_s_barrier();
  gu32* c0 = (gu32*)cnt + rt * 32;
  if (tid == 448) {
    __hip_atomic_fetch_add(c0, 1u, RLX_AGENT);
    for (unsigned spins = 0; spins < (1u << 22); ++spins) {
      if (__hip_atomic_load(c0, RLX_AGENT) >= 4u) break;
      __builtin_amdgcn_s_sleep(1);
    }
  }
  asm volatile("" ::: "memory");
  __builtin_amdgcn_s_barrier();
  asm volatile("" ::: "memory");
  {
    const __amdgpu_buffer_rsrc_t rs = __builtin_amdgcn_make_buffer_rsrc((void*)O, 0, L * D * 2, 0x00020000);
    u32x4 t[5];
#pragma unroll
    for (int i = 0; i < 5; ++i)
      t[i] = __builtin_amdgcn_raw_buffer_load_b128(rs, (unsigned)(rt * 40960 + (w * 5 + i) * 1024 + lane * 16), 0, 16);
#pragma unroll
    for (int i = 0; i < 5; ++i) *(u32x4*)(smem + (w * 5 + i) * 1024 + lane * 16) = t[i];
  }
  __syncthreads();
  f32x4 acc[2][2];
  acc[0][0] = f32x4{}; acc[0][1] = f32x4{}; acc[1][0] = f32x4{}; acc[1][1] = f32x4{};
  float* stt = (float*)smem;
  if (w < 5) {
    const char* obase = smem + lane * 16;
    const char* wl = smem + 2 * CBUF + (2 * w) * WLK * 1024 + lane * 16;
    bf16x8 fo[2][8], fw[2][8];
    auto rd4 = [&](int kp, bf16x8* od, bf16x8* wd) {
#pragma unroll
      for (int kk = 0; kk < 4; ++kk) {
        const int ks = 4 * kp + kk;
        od[2 * kk] = *(const bf16x8*)(obase + ks * 1024);
        od[2 * kk + 1] = *(const bf16x8*)(obase + 20 * 1024 + ks * 1024);
        wd[2 * kk] = ks < WLK ? *(const bf16x8*)(wl + ks * 1024) : w0[ks < WLK ? 0 : ks - WLK];
        wd[2 * kk + 1] = ks < WLK ? *(const bf16x8*)(wl + (WLK + ks) * 1024) : w1[ks < WLK ? 0 : ks - WLK];
      }
    };
    rd4(0, fo[0], fw[0]);
#pragma unroll
    for (int kp = 0; kp < 5; ++kp) {
      __builtin_amdgcn_sched_barrier(0);
      if (kp + 1 < 5) rd4(kp + 1, fo[(kp + 1) & 1], fw[(kp + 1) & 1]);
      __builtin_amdgcn_sched_barrier(0);
#pragma unroll
      for (int kk = 0; kk < 4; ++kk) {
        const bf16x8 o0 = fo[kp & 1][2 * kk], o1 = fo[kp & 1][2 * kk + 1];
        const bf16x8 wa = fw[kp & 1][2 * kk], wb = fw[kp & 1][2 * kk + 1];
        if (FINAL) {
          acc[0][0] = __builtin_amdgcn_mfma_f32_16x16x32_bf16(wa, o0, acc[0][0], 0, 0, 0);
          acc[0][1] = __builtin_amdgcn_mfma_f32_16x16x32_bf16(wa, o1, acc[0][1], 0, 0, 0);
          acc[1][0] = __builtin_amdgcn_mfma_f32_16x16x32_bf16(wb, o0, acc[1][0], 0, 0, 0);
          acc[1][1] = __builtin_amdgcn_mfma_f32_16x16x32_bf16(wb, o1, acc[1][1], 0, 0, 0);
        } else {
          acc[0][0] = __builtin_amdgcn_mfma_f32_16x16x32_bf16(o0, wa, acc[0][0], 0, 0, 0);
          acc[0][1] = __builtin_amdgcn_mfma_f32_16x16x32_bf16(o1, wa, acc[0][1], 0, 0, 0);
          acc[1][0] = __builtin_amdgcn_mfma_f32_16x16x32_bf16(o0, wb, acc[1][0], 0, 0, 0);
          acc[1][1] = __builtin_amdgcn_mfma_f32_16x16x32_bf16(o1, wb, acc[1][1], 0, 0, 0);
        }
      }
    }
  }
  if (!FINAL) __syncthreads();
  if (w < 5) {
    if (FINAL) {
#pragma unroll
      for (int a = 0; a < 2; ++a)
#pragma unroll
        for (int lt = 0; lt < 2; ++lt) {
          const int l = l0 + 16 * lt + i16;
          const int n = 16 * (nt0 + a) + 4 * g;
          float r[4];
#pragma unroll
          for (int e = 0; e < 4; ++e) r[e] = bf2f(xs[a][lt][e]) + rbf(acc[a][lt][e]);
          u32x2 ov = {pk2(r[0], r[1]), pk2(r[2], r[3])};
          *(u32x2*)(xout + (size_t)l * D + n) = ov;
        }
    } else {
#pragma unroll
      for (int a = 0; a < 2; ++a)
#pragma unroll
        for (int lt = 0; lt < 2; ++lt) {
          const int n = 16 * (nt0 + a) + i16;
          const int l = l0 + 16 * lt + 4 * g;
          float r[4];
          r[0] = rbf(bflo(xr[a][lt][0]) + rbf(acc[a][lt][0]));
          r[1] = rbf(bfhi(xr[a][lt][0]) + rbf(acc[a][lt][1]));
          r[2] = rbf(bflo(xr[a][lt][1]) + rbf(acc[a][lt][2]));
          r[3] = rbf(bfhi(xr[a][lt][1]) + rbf(acc[a][lt][3]));
          u32x2 ov = {pk2(r[0], r[1]), pk2(r[2], r[3])};
          *(u32x2*)(xout + (size_t)n * LT + l) = ov;
          const int nl = 16 * (2 * w + a) + i16;
#pragma unroll
          for (int e = 0; e < 4; ++e) stt[(16 * lt + 4 * g + e) * 164 + nl] = r[e];
        }
    }
  }
  if (!FINAL) {
    __syncthreads();
    unsigned* flag = (unsigned*)(smem + 32 * 164 * 4);
    if (tid < 256) {
      const int l = tid >> 3, p8 = tid & 7;
      float v[20];
      float s = 0.f;
#pragma unroll
      for (int e = 0; e < 20; ++e) { v[e] = stt[l * 164 + p8 + 8 * e]; s += v[e]; }
      s += __shfl_xor(s, 1); s += __shfl_xor(s, 2); s += __shfl_xor(s, 4);
      const float mean = s * (1.0f / 160.f);
      float m2 = 0.f;
#pragma unroll
      for (int e = 0; e < 20; ++e) { float a = v[e] - mean; m2 += a * a; }
      m2 += __shfl_xor(m2, 1); m2 += __shfl_xor(m2, 2); m2 += __shfl_xor(m2, 4);
      if (p8 == 0) {
        const unsigned long long pv = (unsigned long long)__float_as_uint(s) | ((unsigned long long)__float_as_uint(m2) << 32);
        __hip_atomic_store((gu64*)(pstats + (size_t)(l0 + l) * 4 + ns), pv, RLX_AGENT);
      }
      asm volatile("s_waitcnt vmcnt(0)" ::: "memory");
    }
    __syncthreads();
    if (tid == 0) *flag = __hip_atomic_fetch_add((gu32*)cnt + (64 + rt) * 32, 1u, RLX_AGENT);
    __syncthreads();
    if (*flag == 3u && tid < 32) {
      float ps[4], pm[4];
      float sum = 0.f;
#pragma unroll
      for (int p = 0; p < 4; ++p) {
        const unsigned long long pv = __hip_atomic_load((gu64*)(pstats + (size_t)(l0 + tid) * 4 + p), RLX_AGENT);
        ps[p] = __uint_as_float((unsigned)pv); pm[p] = __uint_as_float((unsigned)(pv >> 32));
        sum += ps[p];
      }
      const float mean = sum * (1.0f / D);
      float m2 = 0.f;
#pragma unroll
      for (int p = 0; p < 4; ++p) {
        const float dm = ps[p] * (4.0f / D) - mean;
        m2 += pm[p] + dm * dm * (D / 4.0f);
      }
      float mb, sb;
      ln_params(sum, m2, mb, sb);
      f32x2 ov = {mb, sb};
      fstats[l0 + tid] = ov;
    }
  }
}

extern "C" void kernel_launch(void* const* d_in, const int* in_sizes, int n_in, void* d_out, int out_size,
                              void* d_ws, size_t ws_size, hipStream_t stream) {
  const u16* xu = (const u16*)d_in[0];
  const u16* lar = (const u16*)d_in[1];
  const u16* aim = (const u16*)d_in[2];
  const u16* cre = (const u16*)d_in[3];
  const u16* cim = (const u16*)d_in[4];
  const u16* ldt = (const u16*)d_in[5];
  const u16* dsk = (const u16*)d_in[6];
  const u16* wq = (const u16*)d_in[8];
  const u16* wk = (const u16*)d_in[9];
  const u16* wv = (const u16*)d_in[10];
  const u16* wo = (const u16*)d_in[11];
  char* ws = (char*)d_ws;
  u16* out = (u16*)d_out;
  u16* rg = (u16*)(ws + WS_RG);
  u16* xt0 = (u16*)(ws + WS_XT0);
  u16* xt1 = (u16*)(ws + WS_XT1);
  u16* xt2 = (u16*)(ws + WS_XT2);
  u16* xt3 = (u16*)(ws + WS_XT3);
  f32x2* st0 = (f32x2*)(ws + WS_ST0);
  f32x2* st2 = (f32x2*)(ws + WS_ST2);
  u16* Q = (u16*)(ws + WS_Q);
  u16* O = (u16*)(ws + WS_O);
  char* kvf = ws + WS_KV;

  f32x2* sf2 = (f32x2*)(ws + WS_SF2);
  unsigned* cnt = (unsigned*)(ws + WS_CNT);
  prep_kernel<<<PREP_BLOCKS, 256, 0, stream>>>(xu, lar, aim, cre, cim, ldt, wq, wk, wv, wo, ws);
  conv_kernel<<<D, 256, 0, stream>>>(xt0, (const f32x4*)st0, rg, dsk, xt1);
  qkv_kernel<<<256, 512, 0, stream>>>(xt1, ws + WS_WQ, (const float*)(ws + WS_CS), Q, kvf);
  attn_kernel<false><<<256, 512, 0, stream>>>(Q, kvf, O, ws + WS_WO, xt1, xt2, st2, sf2, cnt);
  conv_kernel<<<D, 256, 0, stream>>>(xt2, (const f32x4*)sf2, rg, dsk, xt3);
  qkv_kernel<<<256, 512, 0, stream>>>(xt3, ws + WS_WQ, (const float*)(ws + WS_CS), Q, kvf);
  attn_kernel<true><<<256, 512, 0, stream>>>(Q, kvf, O, ws + WS_WO, xt3, out, st2, sf2, cnt + 128 * 32);
}
```
